```python
import torch

def _softmax(x, axis=-1):
    return torch.softmax(x, dim=axis)
def _gelu(x):
    return torch.nn.functional.gelu(x, approximate="tanh")
def _silu(x):
    return torch.nn.functional.silu(x)
def _sigmoid(x):
    return torch.sigmoid(x)
def _layernorm(x, axis=-1, eps=1e-5):
    m = x.mean(dim=axis, keepdim=True)
    v = x.var(dim=axis, keepdim=True, unbiased=False)
    return (x - m) / torch.sqrt(v + eps)
def _scatter_add(tgt, idx, src, axis=0):
    return tgt.clone().index_add_(axis, idx, src)
def _segment_sum(src, seg, n):
    out = torch.zeros((n,) + src.shape[1:], dtype=src.dtype)
    return out.index_add_(0, seg, src)
def _pad(x, pw):
    flat = tuple(v for pair in reversed(pw) for v in pair)
    return torch.nn.functional.pad(x, flat)

kernel_name = 'interleave_layers__linear_attn_block__moe_arch_mod__prompt'

def setup_inputs():

    _w = (lambda a: (lambda t: t.to(torch.bfloat16) if t.is_floating_point() else t)(torch.as_tensor(a)))
    def _A_setup():
        B, S, D = 1, 2048, 1024
        return {
            "x": _w((lambda *s: torch.randn(*s, dtype=torch.bfloat16) * 0.05)(B, S, D)),
            "wqkv": _w((lambda *s: torch.randn(*s, dtype=torch.bfloat16) * 0.05)(D, 3 * D)),
            "wo": _w((lambda *s: torch.randn(*s, dtype=torch.bfloat16) * 0.05)(D, D)),
        }
    _A_in = _A_setup()

    def _B_setup():
        B, S, D, F, C = 1, 2048, 1024, 3584, 8
        return {
            "x": _w((lambda *s: torch.randn(*s, dtype=torch.bfloat16) * 0.05)(B, S, D)),
            "w_r": _w((lambda *s: torch.randn(*s, dtype=torch.bfloat16) * 0.05)(D)),
            "w_gate": _w((lambda *s: torch.randn(*s, dtype=torch.bfloat16) * 0.05)(D, F)),
            "w_up": _w((lambda *s: torch.randn(*s, dtype=torch.bfloat16) * 0.05)(D, F)),
            "w_down": _w((lambda *s: torch.randn(*s, dtype=torch.bfloat16) * 0.05)(F, D)),
        }
    _B_in = _B_setup()

    return {
        "A_x": _A_in["x"],
        "A_wqkv": _A_in["wqkv"],
        "A_wo": _A_in["wo"],
        "B_x": _B_in["x"],
        "B_w_r": _B_in["w_r"],
        "B_w_gate": _B_in["w_gate"],
        "B_w_up": _B_in["w_up"],
        "B_w_down": _B_in["w_down"],
    }

def reference(A_x, A_wqkv, A_wo, B_x, B_w_r, B_w_gate, B_w_up, B_w_down):
    def _A_ref(x, wqkv, wo):
        B, S, D = x.shape
        H = 32; Dh = D // H
        qkv = (lambda x, *d: x.permute(*d))((x @ wqkv).reshape(B, S, 3, H, Dh), 2, 0, 3, 1, 4)
        q, k, v = qkv[0], qkv[1], qkv[2]
        phi = lambda t: torch.where(t > 0, t + 1, torch.exp(t)) + 1e-6
        pq, pk = phi(q), phi(k)
        kv = torch.einsum("bhsd,bhse->bhde", pk, v)
        z = torch.einsum("bhsd->bhd", pk)
        num = torch.einsum("bhqd,bhde->bhqe", pq, kv)
        den = torch.einsum("bhqd,bhd->bhq", pq, z)[..., None] + 1e-6
        o = num / den
        return (lambda x, *d: x.permute(*d))(o, 0, 2, 1, 3).reshape(B, S, D) @ wo

    def _B_ref(x, w_r, w_gate, w_up, w_down):
        B, S, D = x.shape
        C = 8
        r = x @ w_r
        idx = torch.argsort(-r, axis=-1)[:, :C]
        idx_e = (lambda x, n, axis: torch.repeat_interleave(x, n, dim=axis))(idx.reshape(B, C, 1), D, axis=2)
        x_sel = (lambda x, i, axis: torch.take_along_dim(x, i, dim=axis))(x, idx_e, axis=1)
        g = _silu(x_sel @ w_gate)
        u = x_sel @ w_up
        y = (g * u) @ w_down
        r_sel = _sigmoid((lambda x, i, axis: torch.take_along_dim(x, i, dim=axis))(r, idx, axis=1))
        y = y * r_sel.reshape(B, C, 1)
        b_off = (torch.arange(B) * S).reshape(B, 1)
        flat_idx = (idx + b_off).reshape(B * C)
        zero = (x * 0.0).reshape(B * S, D)
        upd = _scatter_add(zero, flat_idx, y.reshape(B * C, D), axis=0)
        return x + upd.reshape(B, S, D)

    x = A_x
    x = x + _A_ref(_layernorm(x, axis=-1), A_wqkv, A_wo)
    x = x + _B_ref(_layernorm(x, axis=-1), B_w_r, B_w_gate, B_w_up, B_w_down)
    x = x + _A_ref(_layernorm(x, axis=-1), A_wqkv, A_wo)
    x = x + _B_ref(_layernorm(x, axis=-1), B_w_r, B_w_gate, B_w_up, B_w_down)
    return x
```

```cpp
#include <hip/hip_runtime.h>
#include <hip/hip_bf16.h>
#include <stdint.h>

#define S_ 2048
#define D_ 1024
#define H_ 32
#define DH_ 32
#define F_ 3584
#define C_ 8

typedef unsigned short u16;
typedef __attribute__((ext_vector_type(8))) short bf16x8;
typedef __attribute__((ext_vector_type(4))) short bf16x4;
typedef __attribute__((ext_vector_type(4))) float f32x4;
typedef __attribute__((ext_vector_type(4))) unsigned int u32x4;

__device__ __forceinline__ float bf2f(u16 v) { return __uint_as_float(((unsigned)v) << 16); }
__device__ __forceinline__ u16 f2bf(float f) {
    unsigned u = __float_as_uint(f);
    u += 0x7FFFu + ((u >> 16) & 1u);
    return (u16)(u >> 16);
}
__device__ __forceinline__ float rbf(float f) { return bf2f(f2bf(f)); }
__device__ __forceinline__ float lo16(unsigned w) { return __uint_as_float(w << 16); }
__device__ __forceinline__ float hi16(unsigned w) { return __uint_as_float(w & 0xFFFF0000u); }

__device__ __forceinline__ float wave_sum(float v) {
#pragma unroll
    for (int o = 32; o > 0; o >>= 1) v += __shfl_xor(v, o, 64);
    return v;
}

__device__ __forceinline__ void ln_row16(const float (&x)[16], float (&y)[16]) {
    float s = 0.f;
#pragma unroll
    for (int i = 0; i < 16; ++i) s += x[i];
    s = wave_sum(s);
    float mean = s * (1.0f / D_);
    float q = 0.f;
#pragma unroll
    for (int i = 0; i < 16; ++i) { float d = x[i] - mean; q += d * d; }
    q = wave_sum(q);
    float var = rbf(q * (1.0f / D_));
    float mb = rbf(mean);
    float sd = rbf(sqrtf(rbf(var + 1e-5f)));
#pragma unroll
    for (int i = 0; i < 16; ++i) y[i] = rbf(rbf(x[i] - mb) / sd);
}

__device__ __forceinline__ void load_row16(const u16* p, int lane, float (&x)[16]) {
    u32x4 a = *reinterpret_cast<const u32x4*>(p + lane * 8);
    u32x4 b = *reinterpret_cast<const u32x4*>(p + 512 + lane * 8);
#pragma unroll
    for (int i = 0; i < 4; ++i) { x[2 * i] = lo16(a[i]); x[2 * i + 1] = hi16(a[i]); x[8 + 2 * i] = lo16(b[i]); x[8 + 2 * i + 1] = hi16(b[i]); }
}
__device__ __forceinline__ void store_row16(u16* p, int lane, const float (&y)[16]) {
    u32x4 a, b;
#pragma unroll
    for (int i = 0; i < 4; ++i) {
        a[i] = (unsigned)f2bf(y[2 * i]) | ((unsigned)f2bf(y[2 * i + 1]) << 16);
        b[i] = (unsigned)f2bf(y[8 + 2 * i]) | ((unsigned)f2bf(y[8 + 2 * i + 1]) << 16);
    }
    *reinterpret_cast<u32x4*>(p + lane * 8) = a;
    *reinterpret_cast<u32x4*>(p + 512 + lane * 8) = b;
}

__global__ __launch_bounds__(256) void prep_kernel(const u16* __restrict__ x, const u16* __restrict__ wqkv,
                                                   const u16* __restrict__ wo, u16* __restrict__ xln,
                                                   u16* __restrict__ wqkvT, u16* __restrict__ woT) {
    __shared__ u16 tile[64][66];
    int b = blockIdx.x, tid = threadIdx.x;
    if (b < 512) {
        int row = b * 4 + (tid >> 6), lane = tid & 63;
        float xv[16], yv[16];
        load_row16(x + (size_t)row * D_, lane, xv);
        ln_row16(xv, yv);
        store_row16(xln + (size_t)row * D_, lane, yv);
        return;
    }
    b -= 512;
    const u16* src; u16* dst; int N, kt, nt; bool perm;
    if (b < 768) { src = wqkv; dst = wqkvT; N = 3072; kt = b / 48; nt = b % 48; perm = true; }
    else { b -= 768; src = wo; dst = woT; N = 1024; kt = b / 16; nt = b % 16; perm = false; }
    int k0 = kt * 64, n0 = nt * 64;
    for (int i = tid; i < 64 * 64 / 8; i += 256) {
        int kk = i >> 3, nn = (i & 7) * 8;
        u32x4 v = *reinterpret_cast<const u32x4*>(src + (size_t)(k0 + kk) * N + n0 + nn);
#pragma unroll
        for (int j = 0; j < 4; ++j) { tile[kk][nn + 2 * j] = (u16)(v[j] & 0xFFFF); tile[kk][nn + 2 * j + 1] = (u16)(v[j] >> 16); }
    }
    __syncthreads();
    for (int i = tid; i < 64 * 64 / 8; i += 256) {
        int nn = i >> 3, kk = (i & 7) * 8;
        int n = n0 + nn;
        int np;
        if (perm) {
            int part = n >> 10, hd = (n & 1023) >> 5, dd = n & 31;
            np = (hd >> 1) * 192 + (hd & 1) * 96 + part * 32 + dd;
        } else np = n;
        u32x4 v;
#pragma unroll
        for (int j = 0; j < 4; ++j) v[j] = (unsigned)tile[kk + 2 * j][nn] | ((unsigned)tile[kk + 2 * j + 1][nn] << 16);
        *reinterpret_cast<u32x4*>(dst + (size_t)np * D_ + k0 + kk) = v;
    }
}

template <int BN>
struct GemmCfg {
    static constexpr int NT = BN / 2 / 16;
    static constexpr int A_CH = 128 * 8 / 512;
    static constexpr int B_CH = BN * 8 / 512;
    static constexpr int STAGE_BYTES = (128 + BN) * 128;
};

template <int BN>
__device__ __forceinline__ void gemm_main(const u16* __restrict__ A, const u16* __restrict__ Bt, char* smem,
                                          f32x4 (&acc)[2][BN / 32]) {
    using C = GemmCfg<BN>;
    const int tid = threadIdx.x, lane = tid & 63, wid = tid >> 6;
    const int wm = wid >> 1, wn = wid & 1;
    const int fr = lane & 15, fq = lane >> 4;
    u32x4 ra[C::A_CH], rb[C::B_CH];
    auto gload = [&](int kt) {
#pragma unroll
        for (int i = 0; i < C::A_CH; ++i) {
            int c = tid + i * 512; int r = c >> 3, ch = c & 7;
            ra[i] = *reinterpret_cast<const u32x4*>(A + (size_t)r * D_ + kt * 64 + ch * 8);
        }
#pragma unroll
        for (int i = 0; i < C::B_CH; ++i) {
            int c = tid + i * 512; int r = c >> 3, ch = c & 7;
            rb[i] = *reinterpret_cast<const u32x4*>(Bt + (size_t)r * D_ + kt * 64 + ch * 8);
        }
    };
    auto swrite = [&](char* buf) {
#pragma unroll
        for (int i = 0; i < C::A_CH; ++i) {
            int c = tid + i * 512; int r = c >> 3, ch = c & 7;
            *reinterpret_cast<u32x4*>(buf + r * 128 + ((ch ^ (r & 7)) << 4)) = ra[i];
        }
#pragma unroll
        for (int i = 0; i < C::B_CH; ++i) {
            int c = tid + i * 512; int r = c >> 3, ch = c & 7;
            *reinterpret_cast<u32x4*>(buf + 128 * 128 + r * 128 + ((ch ^ (r & 7)) << 4)) = rb[i];
        }
    };
    gload(0);
    swrite(smem);
    __syncthreads();
    for (int kt = 0; kt < 16; ++kt) {
        char* cur = smem + (kt & 1) * C::STAGE_BYTES;
        if (kt + 1 < 16) gload(kt + 1);
#pragma unroll
        for (int ks = 0; ks < 2; ++ks) {
            bf16x8 af[2], bfr[C::NT];
#pragma unroll
            for (int m = 0; m < 2; ++m) {
                int r = wm * 32 + m * 16 + fr; int ch = ks * 4 + fq;
                af[m] = *reinterpret_cast<const bf16x8*>(cur + r * 128 + ((ch ^ (r & 7)) << 4));
            }
#pragma unroll
            for (int n = 0; n < C::NT; ++n) {
                int r = wn * (BN / 2) + n * 16 + fr; int ch = ks * 4 + fq;
                bfr[n] = *reinterpret_cast<const bf16x8*>(cur + 128 * 128 + r * 128 + ((ch ^ (r & 7)) << 4));
            }
#pragma unroll
            for (int m = 0; m < 2; ++m)
#pragma unroll
                for (int n = 0; n < C::NT; ++n)
                    acc[m][n] = __builtin_amdgcn_mfma_f32_16x16x32_bf16(af[m], bfr[n], acc[m][n], 0, 0, 0);
        }
        if (kt + 1 < 16) swrite(smem + ((kt + 1) & 1) * C::STAGE_BYTES);
        __syncthreads();
    }
}

__global__ __launch_bounds__(512) void qkv_kernel(const u16* __restrict__ xln, const u16* __restrict__ wqkvT,
                                                  u16* __restrict__ pq, float* __restrict__ kvp) {
    extern __shared__ __attribute__((aligned(16))) char smem[];
    const int bn = blockIdx.x, bm = blockIdx.y;
    const int tid = threadIdx.x, lane = tid & 63, wid = tid >> 6;
    const int wm = wid >> 1, wn = wid & 1;
    const int fr = lane & 15, fq = lane >> 4;
    f32x4 acc[2][6];
#pragma unroll
    for (int m = 0; m < 2; ++m)
#pragma unroll
        for (int n = 0; n < 6; ++n) acc[m][n] = (f32x4){0.f, 0.f, 0.f, 0.f};
    gemm_main<192>(xln + (size_t)bm * 128 * D_, wqkvT + (size_t)bn * 192 * D_, smem, acc);

    const int head = bn * 2 + wn;
    bf16x4 pk[2][2], vv[2][2];
    float zs[2] = {0.f, 0.f};
#pragma unroll
    for (int m = 0; m < 2; ++m) {
#pragma unroll
        for (int n = 0; n < 2; ++n) {
#pragma unroll
            for (int j = 0; j < 4; ++j) {
                float t = rbf(acc[m][n][j]);
                float p = (t > 0.f) ? rbf(t + 1.f) : rbf(__expf(t));
                p = rbf(p + 1e-6f);
                int row = bm * 128 + wm * 32 + m * 16 + fq * 4 + j;
                pq[(size_t)row * D_ + head * 32 + n * 16 + fr] = f2bf(p);
                float tk = rbf(acc[m][2 + n][j]);
                float pkk = (tk > 0.f) ? rbf(tk + 1.f) : rbf(__expf(tk));
                pkk = rbf(pkk + 1e-6f);
                pk[m][n][j] = (short)f2bf(pkk);
                zs[n] += pkk;
                vv[m][n][j] = (short)f2bf(acc[m][4 + n][j]);
            }
        }
    }
    f32x4 kv[2][2];
#pragma unroll
    for (int i = 0; i < 2; ++i)
#pragma unroll
        for (int j = 0; j < 2; ++j) {
            kv[i][j] = (f32x4){0.f, 0.f, 0.f, 0.f};
#pragma unroll
            for (int m = 0; m < 2; ++m)
                kv[i][j] = __builtin_amdgcn_mfma_f32_16x16x16bf16_1k(pk[m][i], vv[m][j], kv[i][j], 0, 0, 0);
        }
#pragma unroll
    for (int n = 0; n < 2; ++n) { zs[n] += __shfl_xor(zs[n], 16, 64); zs[n] += __shfl_xor(zs[n], 32, 64); }
    float* red = reinterpret_cast<float*>(smem);
#pragma unroll
    for (int i = 0; i < 2; ++i)
#pragma unroll
        for (int j = 0; j < 2; ++j)
#pragma unroll
            for (int r = 0; r < 4; ++r)
                red[wid * 1056 + (i * 16 + fq * 4 + r) * 32 + j * 16 + fr] = kv[i][j][r];
    if (fq == 0) { red[wid * 1056 + 1024 + fr] = zs[0]; red[wid * 1056 + 1024 + 16 + fr] = zs[1]; }
    __syncthreads();
    for (int i = tid; i < 2 * 1056; i += 512) {
        int hh = i / 1056, e = i % 1056;
        float s = red[(0 * 2 + hh) * 1056 + e] + red[(1 * 2 + hh) * 1056 + e] + red[(2 * 2 + hh) * 1056 + e] + red[(3 * 2 + hh) * 1056 + e];
        kvp[((size_t)bm * H_ + bn * 2 + hh) * 1056 + e] = s;
    }
}

__global__ __launch_bounds__(256) void o_kernel(const u16* __restrict__ pq, const float* __restrict__ kvp, u16* __restrict__ o) {
    __shared__ float kv[1056];
    const int head = blockIdx.x, tid = threadIdx.x;
    for (int i = tid; i < 1056; i += 256) {
        float s = 0.f;
#pragma unroll
        for (int b = 0; b < 16; ++b) s += kvp[((size_t)b * H_ + head) * 1056 + i];
        kv[i] = rbf(s);
    }
    __syncthreads();
    const int row = blockIdx.y * 256 + tid;
    float q[32];
    const u32x4* src = reinterpret_cast<const u32x4*>(pq + (size_t)row * D_ + head * 32);
#pragma unroll
    for (int i = 0; i < 4; ++i) {
        u32x4 v = src[i];
#pragma unroll
        for (int j = 0; j < 4; ++j) { q[i * 8 + 2 * j] = lo16(v[j]); q[i * 8 + 2 * j + 1] = hi16(v[j]); }
    }
    float den = 0.f;
#pragma unroll
    for (int d = 0; d < 32; ++d) den += q[d] * kv[1024 + d];
    den = rbf(rbf(den) + 1e-6f);
    u32x4* dst = reinterpret_cast<u32x4*>(o + (size_t)row * D_ + head * 32);
#pragma unroll
    for (int i = 0; i < 4; ++i) {
        u32x4 v;
#pragma unroll
        for (int j = 0; j < 4; ++j) {
            float n0 = 0.f, n1 = 0.f;
#pragma unroll
            for (int d = 0; d < 32; ++d) { n0 += q[d] * kv[d * 32 + i * 8 + 2 * j]; n1 += q[d] * kv[d * 32 + i * 8 + 2 * j + 1]; }
            v[j] = (unsigned)f2bf(rbf(n0) / den) | ((unsigned)f2bf(rbf(n1) / den) << 16);
        }
        dst[i] = v;
    }
}

__global__ __launch_bounds__(512) void wo_kernel(const u16* __restrict__ o, const u16* __restrict__ woT,
                                                 const u16* __restrict__ x, u16* __restrict__ x1) {
    extern __shared__ __attribute__((aligned(16))) char smem[];
    const int bn = blockIdx.x, bm = blockIdx.y;
    const int tid = threadIdx.x, lane = tid & 63, wid = tid >> 6;
    const int wm = wid >> 1, wn = wid & 1;
    const int fr = lane & 15, fq = lane >> 4;
    f32x4 acc[2][2];
#pragma unroll
    for (int m = 0; m < 2; ++m)
#pragma unroll
        for (int n = 0; n < 2; ++n) acc[m][n] = (f32x4){0.f, 0.f, 0.f, 0.f};
    gemm_main<64>(o + (size_t)bm * 128 * D_, woT + (size_t)bn * 64 * D_, smem, acc);
#pragma unroll
    for (int m = 0; m < 2; ++m)
#pragma unroll
        for (int n = 0; n < 2; ++n)
#pragma unroll
            for (int j = 0; j < 4; ++j) {
                size_t idx = (size_t)(bm * 128 + wm * 32 + m * 16 + fq * 4 + j) * D_ + bn * 64 + wn * 32 + n * 16 + fr;
                x1[idx] = f2bf(bf2f(x[idx]) + rbf(acc[m][n][j]));
            }
}

__global__ __launch_bounds__(256) void ln_r_kernel(const u16* __restrict__ x1, const u16* __restrict__ wr,
                                                   u16* __restrict__ xln, float* __restrict__ r) {
    int tid = threadIdx.x, row = blockIdx.x * 4 + (tid >> 6), lane = tid & 63;
    float xv[16], yv[16], wv[16];
    load_row16(x1 + (size_t)row * D_, lane, xv);
    load_row16(wr, lane, wv);
    ln_row16(xv, yv);
    store_row16(xln + (size_t)row * D_, lane, yv);
    float s = 0.f;
#pragma unroll
    for (int i = 0; i < 16; ++i) s += yv[i] * wv[i];
    s = wave_sum(s);
    if (lane == 0) r[row] = rbf(s);
}

__device__ __forceinline__ void top8_wave(const float* __restrict__ r, int lane, int* idx_out) {
    float v[32];
#pragma unroll
    for (int i = 0; i < 32; ++i) v[i] = r[i * 64 + lane];
#pragma unroll
    for (int k = 0; k < 8; ++k) {
        float best = v[0]; int bi = 0;
#pragma unroll
        for (int i = 1; i < 32; ++i) if (v[i] > best) { best = v[i]; bi = i; }
        int gi = bi * 64 + lane;
#pragma unroll
        for (int o = 32; o > 0; o >>= 1) {
            float ob = __shfl_xor(best, o, 64); int oi = __shfl_xor(gi, o, 64);
            if (ob > best || (ob == best && oi < gi)) { best = ob; gi = oi; }
        }
        if (lane == 0) idx_out[k] = gi;
        if ((gi & 63) == lane) {
            int sel = gi >> 6;
#pragma unroll
            for (int i = 0; i < 32; ++i) if (i == sel) v[i] = -3.0e38f;
        }
    }
}

__global__ __launch_bounds__(256) void gateup_kernel(const float* __restrict__ r, const u16* __restrict__ xln,
                                                     const u16* __restrict__ wg, const u16* __restrict__ wu,
                                                     float* __restrict__ h, int* __restrict__ idx_g) {
    __shared__ float xs[D_][8];
    __shared__ float red[4][256];
    __shared__ int idx[8];
    const int tid = threadIdx.x, lane = tid & 63, wid = tid >> 6;
    const int f0 = blockIdx.x * 16;
    const int half = tid & 1, rg = tid >> 1;
    u32x4 g[8], u[8];
#pragma unroll
    for (int p = 0; p < 8; ++p) {
        g[p] = *reinterpret_cast<const u32x4*>(wg + (size_t)(p * 128 + rg) * F_ + f0 + half * 8);
        u[p] = *reinterpret_cast<const u32x4*>(wu + (size_t)(p * 128 + rg) * F_ + f0 + half * 8);
    }
    if (wid == 0) {
        top8_wave(r, lane, idx);
    }
    __syncthreads();
    if (blockIdx.x == 0 && tid < 8) idx_g[tid] = idx[tid];
    for (int i = tid; i < 8 * D_ / 8; i += 256) {
        int t = i >> 7, c = (i & 127) * 8;
        u32x4 v = *reinterpret_cast<const u32x4*>(xln + (size_t)idx[t] * D_ + c);
#pragma unroll
        for (int j = 0; j < 4; ++j) { xs[c + 2 * j][t] = lo16(v[j]); xs[c + 2 * j + 1][t] = hi16(v[j]); }
    }
    __syncthreads();
    float ag[8][8], au[8][8];
#pragma unroll
    for (int t = 0; t < 8; ++t)
#pragma unroll
        for (int c = 0; c < 8; ++c) { ag[t][c] = 0.f; au[t][c] = 0.f; }
#pragma unroll
    for (int p = 0; p < 8; ++p) {
        int d = p * 128 + rg;
        f32x4 xa = *reinterpret_cast<const f32x4*>(&xs[d][0]);
        f32x4 xb = *reinterpret_cast<const f32x4*>(&xs[d][4]);
        float wgf[8], wuf[8];
#pragma unroll
        for (int j = 0; j < 4; ++j) { wgf[2 * j] = lo16(g[p][j]); wgf[2 * j + 1] = hi16(g[p][j]); wuf[2 * j] = lo16(u[p][j]); wuf[2 * j + 1] = hi16(u[p][j]); }
#pragma unroll
        for (int t = 0; t < 8; ++t) {
            float xv = t < 4 ? xa[t] : xb[t - 4];
#pragma unroll
            for (int c = 0; c < 8; ++c) { ag[t][c] += xv * wgf[c]; au[t][c] += xv * wuf[c]; }
        }
    }
#pragma unroll
    for (int t = 0; t < 8; ++t)
#pragma unroll
        for (int c = 0; c < 8; ++c) {
            float a = ag[t][c], b = au[t][c];
#pragma unroll
            for (int o = 2; o < 64; o <<= 1) { a += __shfl_xor(a, o, 64); b += __shfl_xor(b, o, 64); }
            if (lane < 2) { red[wid][(t * 16 + half * 8 + c)] = a; red[wid][128 + (t * 16 + half * 8 + c)] = b; }
        }
    __syncthreads();
    if (tid < 128) {
        float gs = red[0][tid] + red[1][tid] + red[2][tid] + red[3][tid];
        float us = red[0][128 + tid] + red[1][128 + tid] + red[2][128 + tid] + red[3][128 + tid];
        float gb = rbf(gs);
        float sg = rbf(gb / (1.f + __expf(-gb)));
        float hv = rbf(sg * rbf(us));
        int t = tid >> 4, c = tid & 15;
        h[t * F_ + f0 + c] = hv;
    }
}

__global__ __launch_bounds__(256) void down_kernel(const float* __restrict__ h, const u16* __restrict__ wd,
                                                   float* __restrict__ ypart) {
    __shared__ float hs[224][8];
    __shared__ float red[4][8][64];
    const int tid = threadIdx.x, lane = tid & 63, wid = tid >> 6;
    const int n0 = blockIdx.x * 64, fs = blockIdx.y;
    const int c8 = tid & 7, rg = tid >> 3;
    u32x4 w[7];
#pragma unroll
    for (int p = 0; p < 7; ++p)
        w[p] = *reinterpret_cast<const u32x4*>(wd + (size_t)(fs * 224 + p * 32 + rg) * D_ + n0 + c8 * 8);
    for (int i = tid; i < 224 * 8; i += 256) { int t = i / 224, f = i % 224; hs[f][t] = h[t * F_ + fs * 224 + f]; }
    __syncthreads();
    float a[8][8];
#pragma unroll
    for (int t = 0; t < 8; ++t)
#pragma unroll
        for (int c = 0; c < 8; ++c) a[t][c] = 0.f;
#pragma unroll
    for (int p = 0; p < 7; ++p) {
        int f = p * 32 + rg;
        f32x4 xa = *reinterpret_cast<const f32x4*>(&hs[f][0]);
        f32x4 xb = *reinterpret_cast<const f32x4*>(&hs[f][4]);
        float wf[8];
#pragma unroll
        for (int j = 0; j < 4; ++j) { wf[2 * j] = lo16(w[p][j]); wf[2 * j + 1] = hi16(w[p][j]); }
#pragma unroll
        for (int t = 0; t < 8; ++t) {
            float xv = t < 4 ? xa[t] : xb[t - 4];
#pragma unroll
            for (int c = 0; c < 8; ++c) a[t][c] += xv * wf[c];
        }
    }
#pragma unroll
    for (int t = 0; t < 8; ++t)
#pragma unroll
        for (int c = 0; c < 8; ++c) {
            float v = a[t][c];
#pragma unroll
            for (int o = 8; o < 64; o <<= 1) v += __shfl_xor(v, o, 64);
            if (lane < 8) red[wid][t][c8 * 8 + c] = v;
        }
    __syncthreads();
    for (int i = tid; i < 512; i += 256) {
        int t = i >> 6, c = i & 63;
        ypart[((size_t)fs * 8 + t) * D_ + n0 + c] = red[0][t][c] + red[1][t][c] + red[2][t][c] + red[3][t][c];
    }
}

template <bool DO_LN>
__global__ __launch_bounds__(256) void combine_kernel(const u16* __restrict__ x1, const u16* __restrict__ xln,
                                                      const float* __restrict__ r, const int* __restrict__ idx_g,
                                                      const float* __restrict__ ypart, u16* __restrict__ x2,
                                                      u16* __restrict__ xln2) {
    int tid = threadIdx.x, row = blockIdx.x * 4 + (tid >> 6), lane = tid & 63;
    float xv[16], lv[16], yv[16];
    load_row16(x1 + (size_t)row * D_, lane, xv);
    load_row16(xln + (size_t)row * D_, lane, lv);
    int sel = -1;
#pragma unroll
    for (int t = 0; t < 8; ++t) if (idx_g[t] == row) sel = t;
    if (sel >= 0) {
        float rs = rbf(1.f / (1.f + __expf(-r[row])));
#pragma unroll
        for (int i = 0; i < 16; ++i) {
            int col = (i >> 3) * 512 + lane * 8 + (i & 7);
            float s = 0.f;
            for (int fs = 0; fs < 16; ++fs) s += ypart[((size_t)fs * 8 + sel) * D_ + col];
            float y = rbf(rbf(s) * rs);
            lv[i] = rbf(lv[i] + y);
        }
    }
#pragma unroll
    for (int i = 0; i < 16; ++i) xv[i] = rbf(xv[i] + lv[i]);
    store_row16(x2 + (size_t)row * D_, lane, xv);
    if (DO_LN) {
        ln_row16(xv, yv);
        store_row16(xln2 + (size_t)row * D_, lane, yv);
    }
}

extern "C" void kernel_launch(void* const* d_in, const int* in_sizes, int n_in,
                              void* d_out, int out_size, void* d_ws, size_t ws_size,
                              hipStream_t stream) {
    const u16* A_x = (const u16*)d_in[0];
    const u16* wqkv = (const u16*)d_in[1];
    const u16* wo = (const u16*)d_in[2];
    const u16* w_r = (const u16*)d_in[4];
    const u16* w_gate = (const u16*)d_in[5];
    const u16* w_up = (const u16*)d_in[6];
    const u16* w_down = (const u16*)d_in[7];
    u16* out = (u16*)d_out;

    char* ws = (char*)d_ws;
    size_t off = 0;
    auto alloc = [&](size_t bytes) { char* p = ws + off; off += (bytes + 255) & ~(size_t)255; return p; };
    u16* xln = (u16*)alloc((size_t)S_ * D_ * 2);
    u16* wqkvT = (u16*)alloc((size_t)3 * D_ * D_ * 2);
    u16* woT = (u16*)alloc((size_t)D_ * D_ * 2);
    u16* pq = (u16*)alloc((size_t)S_ * D_ * 2);
    float* kvp = (float*)alloc((size_t)16 * H_ * 1056 * 4);
    u16* o = (u16*)alloc((size_t)S_ * D_ * 2);
    u16* x1 = (u16*)alloc((size_t)S_ * D_ * 2);
    u16* x2 = (u16*)alloc((size_t)S_ * D_ * 2);
    float* r = (float*)alloc((size_t)S_ * 4);
    float* h = (float*)alloc((size_t)8 * F_ * 4);
    float* ypart = (float*)alloc((size_t)16 * 8 * D_ * 4);
    int* idx = (int*)alloc(64);

    hipFuncSetAttribute((const void*)qkv_kernel, hipFuncAttributeMaxDynamicSharedMemorySize, 2 * (128 + 192) * 128);
    hipFuncSetAttribute((const void*)wo_kernel, hipFuncAttributeMaxDynamicSharedMemorySize, 2 * (128 + 64) * 128);

    prep_kernel<<<1536, 256, 0, stream>>>(A_x, wqkv, wo, xln, wqkvT, woT);
    const u16* xin = A_x;
    for (int layer = 0; layer < 2; ++layer) {
        u16* xo = (layer == 0) ? x2 : out;
        qkv_kernel<<<dim3(16, 16), 512, 2 * (128 + 192) * 128, stream>>>(xln, wqkvT, pq, kvp);
        o_kernel<<<dim3(32, 8), 256, 0, stream>>>(pq, kvp, o);
        wo_kernel<<<dim3(16, 16), 512, 2 * (128 + 64) * 128, stream>>>(o, woT, xin, x1);
        ln_r_kernel<<<512, 256, 0, stream>>>(x1, w_r, xln, r);
        gateup_kernel<<<224, 256, 0, stream>>>(r, xln, w_gate, w_up, h, idx);
        down_kernel<<<dim3(16, 16), 256, 0, stream>>>(h, w_down, ypart);
        if (layer == 0)
            combine_kernel<true><<<512, 256, 0, stream>>>(x1, xln, r, idx, ypart, xo, xln);
        else
            combine_kernel<false><<<512, 256, 0, stream>>>(x1, xln, r, idx, ypart, xo, xln);
        xin = xo;
    }
}
```

```cpp
#include <hip/hip_runtime.h>
#include <hip/hip_bf16.h>
#include <stdint.h>

#define S_ 2048
#define D_ 1024
#define H_ 32
#define DH_ 32
#define F_ 3584
#define C_ 8

typedef unsigned short u16;
typedef __attribute__((ext_vector_type(8))) short bf16x8;
typedef __attribute__((ext_vector_type(4))) short bf16x4;
typedef __attribute__((ext_vector_type(4))) float f32x4;
typedef __attribute__((ext_vector_type(4))) unsigned int u32x4;

__device__ __forceinline__ float bf2f(u16 v) { return __uint_as_float(((unsigned)v) << 16); }
__device__ __forceinline__ u16 f2bf(float f) {
    __bf16 b = (__bf16)f;
    return __builtin_bit_cast(u16, b);
}
__device__ __forceinline__ float rbf(float f) { return bf2f(f2bf(f)); }
__device__ __forceinline__ float lo16(unsigned w) { return __uint_as_float(w << 16); }
__device__ __forceinline__ float hi16(unsigned w) { return __uint_as_float(w & 0xFFFF0000u); }

template <int CTRL, int RM>
__device__ __forceinline__ float dpp_addf(float v) {
    return v + __int_as_float(__builtin_amdgcn_update_dpp(0, __float_as_int(v), CTRL, RM, 0xf, true));
}
__device__ __forceinline__ float wave_sum(float v) {
    v = dpp_addf<0x111, 0xf>(v);
    v = dpp_addf<0x112, 0xf>(v);
    v = dpp_addf<0x114, 0xf>(v);
    v = dpp_addf<0x118, 0xf>(v);
    v = dpp_addf<0x142, 0xa>(v);
    v = dpp_addf<0x143, 0xc>(v);
    return __int_as_float(__builtin_amdgcn_readlane(__float_as_int(v), 63));
}
template <int CTRL, int RM>
__device__ __forceinline__ unsigned dpp_maxu(unsigned v) {
    unsigned o = (unsigned)__builtin_amdgcn_update_dpp(0, (int)v, CTRL, RM, 0xf, true);
    return v > o ? v : o;
}
__device__ __forceinline__ unsigned wave_maxu(unsigned v) {
    v = dpp_maxu<0x111, 0xf>(v);
    v = dpp_maxu<0x112, 0xf>(v);
    v = dpp_maxu<0x114, 0xf>(v);
    v = dpp_maxu<0x118, 0xf>(v);
    v = dpp_maxu<0x142, 0xa>(v);
    v = dpp_maxu<0x143, 0xc>(v);
    return (unsigned)__builtin_amdgcn_readlane((int)v, 63);
}

__device__ __forceinline__ void ln_row16(const float (&x)[16], float (&y)[16]) {
    float s = 0.f;
#pragma unroll
    for (int i = 0; i < 16; ++i) s += x[i];
    s = wave_sum(s);
    float mean = s * (1.0f / D_);
    float q = 0.f;
#pragma unroll
    for (int i = 0; i < 16; ++i) { float d = x[i] - mean; q += d * d; }
    q = wave_sum(q);
    float var = rbf(q * (1.0f / D_));
    float mb = rbf(mean);
    float inv = 1.0f / rbf(sqrtf(rbf(var + 1e-5f)));
#pragma unroll
    for (int i = 0; i < 16; ++i) y[i] = rbf(rbf(x[i] - mb) * inv);
}

__device__ __forceinline__ void load_row16(const u16* p, int lane, float (&x)[16]) {
    u32x4 a = *reinterpret_cast<const u32x4*>(p + lane * 8);
    u32x4 b = *reinterpret_cast<const u32x4*>(p + 512 + lane * 8);
#pragma unroll
    for (int i = 0; i < 4; ++i) { x[2 * i] = lo16(a[i]); x[2 * i + 1] = hi16(a[i]); x[8 + 2 * i] = lo16(b[i]); x[8 + 2 * i + 1] = hi16(b[i]); }
}
__device__ __forceinline__ void store_row16(u16* p, int lane, const float (&y)[16]) {
    u32x4 a, b;
#pragma unroll
    for (int i = 0; i < 4; ++i) {
        a[i] = (unsigned)f2bf(y[2 * i]) | ((unsigned)f2bf(y[2 * i + 1]) << 16);
        b[i] = (unsigned)f2bf(y[8 + 2 * i]) | ((unsigned)f2bf(y[8 + 2 * i + 1]) << 16);
    }
    *reinterpret_cast<u32x4*>(p + lane * 8) = a;
    *reinterpret_cast<u32x4*>(p + 512 + lane * 8) = b;
}

__global__ __launch_bounds__(256) void prep_kernel(const u16* __restrict__ x, const u16* __restrict__ wqkv,
                                                   const u16* __restrict__ wo, u16* __restrict__ xln,
                                                   u16* __restrict__ wqkvT, u16* __restrict__ woT, int* __restrict__ cnt) {
    __shared__ u16 tile[64][66];
    int b = blockIdx.x, tid = threadIdx.x;
    if (b == 0 && tid < 128) cnt[tid] = 0;
    if (b < 512) {
        int row = b * 4 + (tid >> 6), lane = tid & 63;
        float xv[16], yv[16];
        load_row16(x + (size_t)row * D_, lane, xv);
        ln_row16(xv, yv);
        store_row16(xln + (size_t)row * D_, lane, yv);
        return;
    }
    b -= 512;
    const u16* src; u16* dst; int N, kt, nt; bool perm;
    if (b < 768) { src = wqkv; dst = wqkvT; N = 3072; kt = b / 48; nt = b % 48; perm = true; }
    else { b -= 768; src = wo; dst = woT; N = 1024; kt = b / 16; nt = b % 16; perm = false; }
    int k0 = kt * 64, n0 = nt * 64;
    for (int i = tid; i < 64 * 64 / 8; i += 256) {
        int kk = i >> 3, nn = (i & 7) * 8;
        u32x4 v = *reinterpret_cast<const u32x4*>(src + (size_t)(k0 + kk) * N + n0 + nn);
#pragma unroll
        for (int j = 0; j < 4; ++j) { tile[kk][nn + 2 * j] = (u16)(v[j] & 0xFFFF); tile[kk][nn + 2 * j + 1] = (u16)(v[j] >> 16); }
    }
    __syncthreads();
    for (int i = tid; i < 64 * 64 / 8; i += 256) {
        int nn = i >> 3, kk = (i & 7) * 8;
        int n = n0 + nn;
        int np;
        if (perm) {
            int part = n >> 10, hd = (n & 1023) >> 5, dd = n & 31;
            np = (hd >> 1) * 192 + (hd & 1) * 96 + part * 32 + dd;
        } else np = n;
        u32x4 v;
        if (perm) {
#pragma unroll
            for (int j = 0; j < 4; ++j) v[j] = (unsigned)tile[kk + 2 * j][nn] | ((unsigned)tile[kk + 2 * j + 1][nn] << 16);
        } else {
#pragma unroll
            for (int j = 0; j < 4; ++j) v[j] = (unsigned)tile[kk + 2 * j][nn] | ((unsigned)tile[kk + 2 * j + 1][nn] << 16);
        }
        *reinterpret_cast<u32x4*>(dst + (size_t)np * D_ + k0 + kk) = v;
    }
}

#define TSK(i) do { } while (0)
#define WAITV(n) asm volatile("s_waitcnt vmcnt(%0)" ::"n"(n) : "memory")
#define RBAR() do { asm volatile("s_waitcnt lgkmcnt(0)" ::: "memory"); __builtin_amdgcn_s_barrier(); } while (0)
template <int BN, int NST>
__device__ __forceinline__ void gemm_main(const u16* __restrict__ A, const u16* __restrict__ Bt, char* smem,
                                          f32x4 (&out)[2][BN / 32]) {
    constexpr int NT = BN / 32;
    constexpr int STAGE = (128 + BN) * 128;
    constexpr int A_CH = 2, B_CH = BN * 8 / 512, G = A_CH + B_CH;
    const int tid = threadIdx.x, lane = tid & 63, wid = tid >> 6;
    const int wk = wid >> 2, wm = (wid >> 1) & 1, wn = wid & 1;
    const int fr = lane & 15, fq = lane >> 4;
    f32x4 acc[4][NT];
#pragma unroll
    for (int m = 0; m < 4; ++m)
#pragma unroll
        for (int n = 0; n < NT; ++n) acc[m][n] = (f32x4){0.f, 0.f, 0.f, 0.f};
    const u16* pa[A_CH]; const u16* pb[B_CH];
#pragma unroll
    for (int i = 0; i < A_CH; ++i) { int c = tid + i * 512, r = c >> 3, ph = c & 7; pa[i] = A + (size_t)r * D_ + ((ph ^ ((r >> 1) & 7)) << 3); }
#pragma unroll
    for (int i = 0; i < B_CH; ++i) { int c = tid + i * 512, r = c >> 3, ph = c & 7; pb[i] = Bt + (size_t)r * D_ + ((ph ^ ((r >> 1) & 7)) << 3); }
    const int ch = wk * 4 + fq;
    const int offA = (wm * 64 + fr) * 128 + ((ch ^ ((fr >> 1) & 7)) << 4);
    const int offB = 16384 + (wn * (BN / 2) + fr) * 128 + ((ch ^ ((fr >> 1) & 7)) << 4);
    auto issue = [&](int kt) {
        char* buf = smem + (kt % NST) * STAGE + wid * 1024;
#pragma unroll
        for (int i = 0; i < A_CH; ++i)
            __builtin_amdgcn_global_load_lds((const unsigned*)(pa[i] + kt * 64), (unsigned*)(buf + i * 8192), 16, 0, 0);
#pragma unroll
        for (int i = 0; i < B_CH; ++i)
            __builtin_amdgcn_global_load_lds((const unsigned*)(pb[i] + kt * 64), (unsigned*)(buf + 16384 + i * 8192), 16, 0, 0);
    };
    auto compute = [&](int kt) {
        const char* cur = smem + (kt % NST) * STAGE;
        bf16x8 af[4], bfr[NT];
#pragma unroll
        for (int m = 0; m < 4; ++m) af[m] = *reinterpret_cast<const bf16x8*>(cur + offA + m * 2048);
#pragma unroll
        for (int n = 0; n < NT; ++n) bfr[n] = *reinterpret_cast<const bf16x8*>(cur + offB + n * 2048);
#pragma unroll
        for (int n = 0; n < NT; ++n)
#pragma unroll
            for (int m = 0; m < 4; ++m)
                acc[m][n] = __builtin_amdgcn_mfma_f32_16x16x32_bf16(af[m], bfr[n], acc[m][n], 0, 0, 0);
    };
#pragma unroll
    for (int t = 0; t < NST - 1; ++t) issue(t);
    for (int kt = 0; kt < 16 - (NST - 1); ++kt) {
        WAITV((NST - 2) * G);
        RBAR();
        issue(kt + NST - 1);
        compute(kt);
    }
#pragma unroll
    for (int j = 0; j < NST - 1; ++j) {
        WAITV((NST - 2 - j) * G);
        RBAR();
        compute(16 - (NST - 1) + j);
    }
    RBAR();
    f32x4* xch = reinterpret_cast<f32x4*>(smem);
    const int myslot = (wk * 4 + wm * 2 + wn), other = ((wk ^ 1) * 4 + wm * 2 + wn);
#pragma unroll
    for (int m = 0; m < 2; ++m)
#pragma unroll
        for (int n = 0; n < NT; ++n) xch[((myslot * 2 + m) * NT + n) * 64 + lane] = wk ? acc[m][n] : acc[2 + m][n];
    __syncthreads();
#pragma unroll
    for (int m = 0; m < 2; ++m)
#pragma unroll
        for (int n = 0; n < NT; ++n) out[m][n] = (wk ? acc[2 + m][n] : acc[m][n]) + xch[((other * 2 + m) * NT + n) * 64 + lane];
    __syncthreads();
}

__global__ __launch_bounds__(512) void qkv_kernel(const u16* __restrict__ xln, const u16* __restrict__ wqkvT,
                                                  u16* __restrict__ pq, float* __restrict__ kvp) {
    extern __shared__ __attribute__((aligned(16))) char smem[];
    const int xcd = blockIdx.x & 7, jj = blockIdx.x >> 3;
    const int bm = (xcd & 3) * 4 + (jj & 3), bn = (xcd >> 2) * 8 + (jj >> 2);
    const int tid = threadIdx.x, lane = tid & 63, wid = tid >> 6;
    const int wq = (wid >> 2) + ((wid >> 1) & 1) * 2;
    const int wn = wid & 1;
    const int fr = lane & 15, fq = lane >> 4;
    f32x4 acc[2][6];
    gemm_main<192, 4>(xln + (size_t)bm * 128 * D_, wqkvT + (size_t)bn * 192 * D_, smem, acc);

    u16* qs = reinterpret_cast<u16*>(smem + 40960);
    bf16x4 pk[2][2], vv[2][2];
    float zs[2] = {0.f, 0.f};
#pragma unroll
    for (int m = 0; m < 2; ++m) {
#pragma unroll
        for (int n = 0; n < 2; ++n) {
#pragma unroll
            for (int j = 0; j < 4; ++j) {
                float t = rbf(acc[m][n][j]);
                float p = (t > 0.f) ? rbf(t + 1.f) : rbf(__expf(t));
                p = rbf(p + 1e-6f);
                qs[(wq * 32 + m * 16 + fq * 4 + j) * 64 + wn * 32 + n * 16 + fr] = f2bf(p);
                float tk = rbf(acc[m][2 + n][j]);
                float pkk = (tk > 0.f) ? rbf(tk + 1.f) : rbf(__expf(tk));
                pkk = rbf(pkk + 1e-6f);
                pk[m][n][j] = (short)f2bf(pkk);
                zs[n] += pkk;
                vv[m][n][j] = (short)f2bf(acc[m][4 + n][j]);
            }
        }
    }
    f32x4 kv[2][2];
#pragma unroll
    for (int i = 0; i < 2; ++i)
#pragma unroll
        for (int j = 0; j < 2; ++j) {
            kv[i][j] = (f32x4){0.f, 0.f, 0.f, 0.f};
#pragma unroll
            for (int m = 0; m < 2; ++m)
                kv[i][j] = __builtin_amdgcn_mfma_f32_16x16x16bf16_1k(pk[m][i], vv[m][j], kv[i][j], 0, 0, 0);
        }
#pragma unroll
    for (int n = 0; n < 2; ++n) { zs[n] += __shfl_xor(zs[n], 16, 64); zs[n] += __shfl_xor(zs[n], 32, 64); }
    float* red = reinterpret_cast<float*>(smem);
#pragma unroll
    for (int i = 0; i < 2; ++i)
#pragma unroll
        for (int j = 0; j < 2; ++j)
#pragma unroll
            for (int r = 0; r < 4; ++r)
                red[(wq * 2 + wn) * 1056 + (i * 16 + fq * 4 + r) * 32 + j * 16 + fr] = kv[i][j][r];
    if (fq == 0) { red[(wq * 2 + wn) * 1056 + 1024 + fr] = zs[0]; red[(wq * 2 + wn) * 1056 + 1024 + 16 + fr] = zs[1]; }
    __syncthreads();
    for (int i = tid; i < 2 * 1056; i += 512) {
        int hh = i / 1056, e = i % 1056;
        float s = red[(0 * 2 + hh) * 1056 + e] + red[(1 * 2 + hh) * 1056 + e] + red[(2 * 2 + hh) * 1056 + e] + red[(3 * 2 + hh) * 1056 + e];
        kvp[((size_t)bm * H_ + bn * 2 + hh) * 1056 + e] = s;
    }
#pragma unroll
    for (int i = 0; i < 2; ++i) {
        int c = tid + i * 512, r = c >> 3, ch = c & 7;
        *reinterpret_cast<u32x4*>(pq + (size_t)(bm * 128 + r) * D_ + bn * 64 + ch * 8) = *reinterpret_cast<const u32x4*>(qs + r * 64 + ch * 8);
    }
}

__global__ __launch_bounds__(256) void o_kernel(const u16* __restrict__ pq, const float* __restrict__ kvp, u16* __restrict__ o) {
    __shared__ __attribute__((aligned(16))) u16 kvT[32][40];
    __shared__ __attribute__((aligned(16))) u16 zb[32];
    const int head = blockIdx.x, tid = threadIdx.x, lane = tid & 63, wid = tid >> 6;
    const int fr = lane & 15, fq = lane >> 4;
    u32x4 qv[4];
#pragma unroll
    for (int it = 0; it < 4; ++it)
        qv[it] = *reinterpret_cast<const u32x4*>(pq + (size_t)(blockIdx.y * 256 + wid * 64 + it * 16 + fr) * D_ + head * 32 + fq * 8);
    float v[5][16];
#pragma unroll
    for (int it = 0; it < 5; ++it) {
        int i = tid + it * 256; i = i < 1056 ? i : 1055;
#pragma unroll
        for (int b = 0; b < 16; ++b) v[it][b] = kvp[((size_t)b * H_ + head) * 1056 + i];
    }
#pragma unroll
    for (int it = 0; it < 5; ++it) {
        int i = tid + it * 256;
        if (i < 1056) {
            float s = 0.f;
#pragma unroll
            for (int b = 0; b < 16; ++b) s += v[it][b];
            if (i < 1024) kvT[i & 31][i >> 5] = f2bf(s); else zb[i - 1024] = f2bf(s);
        }
    }
    __syncthreads();
    bf16x8 ka = *reinterpret_cast<const bf16x8*>(&kvT[fr][fq * 8]);
    bf16x8 kb = *reinterpret_cast<const bf16x8*>(&kvT[16 + fr][fq * 8]);
    bf16x8 za = *reinterpret_cast<const bf16x8*>(&zb[fq * 8]);
#pragma unroll
    for (int it = 0; it < 4; ++it) {
        int row = blockIdx.y * 256 + wid * 64 + it * 16 + fr;
        bf16x8 qf = __builtin_bit_cast(bf16x8, qv[it]);
        f32x4 z4 = {0.f, 0.f, 0.f, 0.f};
        f32x4 cz = __builtin_amdgcn_mfma_f32_16x16x32_bf16(za, qf, z4, 0, 0, 0);
        f32x4 c0 = __builtin_amdgcn_mfma_f32_16x16x32_bf16(ka, qf, z4, 0, 0, 0);
        f32x4 c1 = __builtin_amdgcn_mfma_f32_16x16x32_bf16(kb, qf, z4, 0, 0, 0);
        float den = rbf(rbf(cz[0]) + 1e-6f);
        uint2 w0, w1;
        w0.x = (unsigned)f2bf(rbf(c0[0]) / den) | ((unsigned)f2bf(rbf(c0[1]) / den) << 16);
        w0.y = (unsigned)f2bf(rbf(c0[2]) / den) | ((unsigned)f2bf(rbf(c0[3]) / den) << 16);
        w1.x = (unsigned)f2bf(rbf(c1[0]) / den) | ((unsigned)f2bf(rbf(c1[1]) / den) << 16);
        w1.y = (unsigned)f2bf(rbf(c1[2]) / den) | ((unsigned)f2bf(rbf(c1[3]) / den) << 16);
        *reinterpret_cast<uint2*>(o + (size_t)row * D_ + head * 32 + fq * 4) = w0;
        *reinterpret_cast<uint2*>(o + (size_t)row * D_ + head * 32 + 16 + fq * 4) = w1;
    }
}

__global__ __launch_bounds__(512) void wo_kernel(const u16* __restrict__ o, const u16* __restrict__ woT,
                                                 const u16* __restrict__ x, u16* __restrict__ x1) {
    extern __shared__ __attribute__((aligned(16))) char smem[];
    const int xcd = blockIdx.x & 7, jj = blockIdx.x >> 3;
    const int bm = (xcd & 3) * 4 + (jj & 3), bn = (xcd >> 2) * 8 + (jj >> 2);
    const int tid = threadIdx.x, lane = tid & 63, wid = tid >> 6;
    const int wm = (wid >> 2) + ((wid >> 1) & 1) * 2, wn = wid & 1;
    const int fr = lane & 15, fq = lane >> 4;
    f32x4 acc[2][2];
    gemm_main<64, 6>(o + (size_t)bm * 128 * D_, woT + (size_t)bn * 64 * D_, smem, acc);
    float* cs = reinterpret_cast<float*>(smem);
#pragma unroll
    for (int m = 0; m < 2; ++m)
#pragma unroll
        for (int n = 0; n < 2; ++n)
#pragma unroll
            for (int j = 0; j < 4; ++j)
                cs[(wm * 32 + m * 16 + fq * 4 + j) * 68 + wn * 32 + n * 16 + fr] = acc[m][n][j];
    __syncthreads();
#pragma unroll
    for (int i = 0; i < 2; ++i) {
        int c = tid + i * 512, r = c >> 3, ch = c & 7;
        size_t idx = (size_t)(bm * 128 + r) * D_ + bn * 64 + ch * 8;
        u32x4 xv = *reinterpret_cast<const u32x4*>(x + idx);
        u32x4 ov;
#pragma unroll
        for (int j = 0; j < 4; ++j) {
            float a0 = rbf(cs[r * 68 + ch * 8 + 2 * j]), a1 = rbf(cs[r * 68 + ch * 8 + 2 * j + 1]);
            ov[j] = (unsigned)f2bf(lo16(xv[j]) + a0) | ((unsigned)f2bf(hi16(xv[j]) + a1) << 16);
        }
        *reinterpret_cast<u32x4*>(x1 + idx) = ov;
    }
}

template <bool DO_LN>
__global__ __launch_bounds__(256) void ln_r_kernel(const u16* __restrict__ x1, const u16* __restrict__ wr,
                                                   float* __restrict__ r, u16* __restrict__ x2, u16* __restrict__ xln2) {
    int tid = threadIdx.x, row = blockIdx.x * 4 + (tid >> 6), lane = tid & 63;
    float xv[16], yv[16], wv[16];
    load_row16(x1 + (size_t)row * D_, lane, xv);
    load_row16(wr, lane, wv);
    ln_row16(xv, yv);
    float s = 0.f;
#pragma unroll
    for (int i = 0; i < 16; ++i) s += yv[i] * wv[i];
    s = wave_sum(s);
    if (lane == 0) r[row] = rbf(s);
#pragma unroll
    for (int i = 0; i < 16; ++i) xv[i] = rbf(xv[i] + yv[i]);
    store_row16(x2 + (size_t)row * D_, lane, xv);
    if (DO_LN) {
        ln_row16(xv, yv);
        store_row16(xln2 + (size_t)row * D_, lane, yv);
    }
}

__device__ __forceinline__ void top8_wave(const float* __restrict__ r, int lane, int* idx_out) {
    unsigned v[32];
#pragma unroll
    for (int i = 0; i < 32; ++i) {
        unsigned b = __float_as_uint(r[i * 64 + lane]) >> 16;
        b = (b & 0x8000u) ? (~b & 0xFFFFu) : (b | 0x8000u);
        v[i] = (b << 16) | (65535u - (unsigned)(i * 64 + lane));
    }
#pragma unroll
    for (int k = 0; k < 8; ++k) {
        unsigned best = v[0];
#pragma unroll
        for (int i = 1; i < 32; ++i) best = v[i] > best ? v[i] : best;
        best = wave_maxu(best);
        if (lane == 0) idx_out[k] = (int)(65535u - (best & 0xFFFFu));
#pragma unroll
        for (int i = 0; i < 32; ++i) v[i] = (v[i] == best) ? 0u : v[i];
    }
}

typedef __attribute__((ext_vector_type(4))) short s16x4v;
__device__ __forceinline__ s16x4v tr_read(const void* p) {
    return __builtin_amdgcn_ds_read_tr16_b64_v4i16((__attribute__((address_space(3))) s16x4v*)p);
}

__global__ __launch_bounds__(256) void gateup_kernel(const float* __restrict__ r, const u16* __restrict__ x1,
                                                     const u16* __restrict__ wg, const u16* __restrict__ wu,
                                                     float* __restrict__ gp, int* __restrict__ idx_g) {
    __shared__ __attribute__((aligned(16))) char smem[65536 + 16 * 264 * 2 + 64];
    char* Wgs = smem; char* Wus = smem + 32768;
    u16* xs = reinterpret_cast<u16*>(smem + 65536);
    int* idx = reinterpret_cast<int*>(smem + 65536 + 16 * 264 * 2);
    const int tid = threadIdx.x, lane = tid & 63, wid = tid >> 6;
    const int fr = lane & 15, fq = lane >> 4;
    const int f0 = blockIdx.x * 64, dq = blockIdx.y;
    if (wid > 0) {
        for (int i = wid - 1; i < 64; i += 3) {
            int mi = i >> 5, ii = i & 31;
            const u16* w = mi ? wu : wg;
            int row = ii * 8 + (lane >> 3);
            __builtin_amdgcn_global_load_lds((const unsigned*)(w + (size_t)(dq * 256 + row) * F_ + f0 + (lane & 7) * 8),
                                             (unsigned*)(smem + mi * 32768 + ii * 1024), 16, 0, 0);
        }
    } else {
        top8_wave(r, lane, idx);
        if (blockIdx.x == 0 && dq == 0 && lane < 8) idx_g[lane] = idx[lane];
#pragma unroll
        for (int i = 0; i < 4; ++i) {
            int t = 8 + i * 2 + (lane >> 5), c = (lane & 31) * 8;
            *reinterpret_cast<u32x4*>(xs + t * 264 + c) = (u32x4){0u, 0u, 0u, 0u};
        }
    }
    __syncthreads();
    {
        u32x4 ra[2], rb[2];
#pragma unroll
        for (int tt = 0; tt < 2; ++tt) {
            const u16* rp = x1 + (size_t)idx[wid * 2 + tt] * D_;
            ra[tt] = *reinterpret_cast<const u32x4*>(rp + lane * 8);
            rb[tt] = *reinterpret_cast<const u32x4*>(rp + 512 + lane * 8);
        }
#pragma unroll
        for (int tt = 0; tt < 2; ++tt) {
            int t = wid * 2 + tt;
            float xv[16], yv[16];
#pragma unroll
            for (int i = 0; i < 4; ++i) { xv[2 * i] = lo16(ra[tt][i]); xv[2 * i + 1] = hi16(ra[tt][i]); xv[8 + 2 * i] = lo16(rb[tt][i]); xv[8 + 2 * i + 1] = hi16(rb[tt][i]); }
            ln_row16(xv, yv);
            int e0 = lane * 8 - dq * 256, e1 = 512 + lane * 8 - dq * 256;
            u32x4 pa, pb;
#pragma unroll
            for (int i = 0; i < 4; ++i) {
                pa[i] = (unsigned)f2bf(yv[2 * i]) | ((unsigned)f2bf(yv[2 * i + 1]) << 16);
                pb[i] = (unsigned)f2bf(yv[8 + 2 * i]) | ((unsigned)f2bf(yv[8 + 2 * i + 1]) << 16);
            }
            if (e0 >= 0 && e0 < 256) *reinterpret_cast<u32x4*>(xs + t * 264 + e0) = pa;
            if (e1 >= 0 && e1 < 256) *reinterpret_cast<u32x4*>(xs + t * 264 + e1) = pb;
        }
    }
    asm volatile("s_waitcnt vmcnt(0)" ::: "memory");
    __syncthreads();
    f32x4 ag = {0.f, 0.f, 0.f, 0.f}, au = {0.f, 0.f, 0.f, 0.f};
    const int q = (lane >> 2) & 3, pp = lane & 3;
#pragma unroll
    for (int ks = 0; ks < 8; ++ks) {
        int k0 = ks * 32;
        bf16x8 af = *reinterpret_cast<const bf16x8*>(xs + fr * 264 + k0 + fq * 8);
        int ro = (k0 + fq * 8 + q) * 128 + wid * 32 + pp * 8;
        s16x4v g0 = tr_read(Wgs + ro), g1 = tr_read(Wgs + ro + 512);
        s16x4v u0 = tr_read(Wus + ro), u1 = tr_read(Wus + ro + 512);
        bf16x8 bg = {g0[0], g0[1], g0[2], g0[3], g1[0], g1[1], g1[2], g1[3]};
        bf16x8 bu = {u0[0], u0[1], u0[2], u0[3], u1[0], u1[1], u1[2], u1[3]};
        ag = __builtin_amdgcn_mfma_f32_16x16x32_bf16(af, bg, ag, 0, 0, 0);
        au = __builtin_amdgcn_mfma_f32_16x16x32_bf16(af, bu, au, 0, 0, 0);
    }
    if (fq < 2) {
#pragma unroll
        for (int j = 0; j < 4; ++j) {
            gp[((size_t)(dq * 2 + 0) * 8 + fq * 4 + j) * F_ + f0 + wid * 16 + fr] = ag[j];
            gp[((size_t)(dq * 2 + 1) * 8 + fq * 4 + j) * F_ + f0 + wid * 16 + fr] = au[j];
        }
    }
}

__global__ __launch_bounds__(256) void down_kernel(const float* __restrict__ gp, const u16* __restrict__ wd,
                                                   float* __restrict__ ypart) {
    __shared__ __attribute__((aligned(16))) char smem[28672 + 16 * 232 * 2];
    char* Ws = smem; u16* hs = reinterpret_cast<u16*>(smem + 28672);
    const int tid = threadIdx.x, lane = tid & 63, wid = tid >> 6;
    const int fr = lane & 15, fq = lane >> 4;
    const int n0 = blockIdx.x * 64, fs = blockIdx.y;
    float hv[7];
#pragma unroll
    for (int i = 0; i < 7; ++i) {
        int e = tid + i * 256; int t = e / 224, f = e % 224;
        float gs = 0.f, us = 0.f;
#pragma unroll
        for (int d = 0; d < 4; ++d) {
            gs += gp[((size_t)(d * 2 + 0) * 8 + t) * F_ + fs * 224 + f];
            us += gp[((size_t)(d * 2 + 1) * 8 + t) * F_ + fs * 224 + f];
        }
        float gb = rbf(gs);
        float sg = rbf(gb / (1.f + __expf(-gb)));
        hv[i] = sg * rbf(us);
    }
#pragma unroll
    for (int i = 0; i < 7; ++i) {
        int c = tid + i * 256;
        __builtin_amdgcn_global_load_lds((const unsigned*)(wd + (size_t)(fs * 224 + (c >> 3)) * D_ + n0 + (c & 7) * 8), (unsigned*)(Ws + i * 4096 + wid * 1024), 16, 0, 0);
    }
#pragma unroll
    for (int i = 0; i < 7; ++i) {
        int e = tid + i * 256; int t = e / 224, f = e % 224;
        hs[t * 232 + f] = f2bf(hv[i]);
        hs[(8 + t) * 232 + f] = 0;
    }
    asm volatile("s_waitcnt vmcnt(0)" ::: "memory");
    __syncthreads();
    f32x4 a = {0.f, 0.f, 0.f, 0.f};
#pragma unroll
    for (int ks = 0; ks < 7; ++ks) {
        int k0 = ks * 32;
        bf16x8 af = *reinterpret_cast<const bf16x8*>(hs + fr * 232 + k0 + fq * 8);
        int q = (lane >> 2) & 3, p = lane & 3;
        int ro = (k0 + fq * 8 + q) * 128 + wid * 32 + p * 8;
        s16x4v b0 = tr_read(Ws + ro), b1 = tr_read(Ws + ro + 512);
        bf16x8 bf = {b0[0], b0[1], b0[2], b0[3], b1[0], b1[1], b1[2], b1[3]};
        a = __builtin_amdgcn_mfma_f32_16x16x32_bf16(af, bf, a, 0, 0, 0);
    }
    if (fq < 2) {
#pragma unroll
        for (int j = 0; j < 4; ++j) ypart[((size_t)fs * 8 + fq * 4 + j) * D_ + n0 + wid * 16 + fr] = a[j];
    }
}

template <bool DO_LN>
__global__ __launch_bounds__(64) void fixup_kernel(const u16* __restrict__ x1, const float* __restrict__ r,
                                                   const int* __restrict__ idx_g, const float* __restrict__ ypart,
                                                   u16* __restrict__ x2, u16* __restrict__ xln2) {
    const int sel = blockIdx.x, lane = threadIdx.x;
    f32x4 yp[16][4];
#pragma unroll
    for (int f = 0; f < 16; ++f) {
        const float* y = ypart + ((size_t)f * 8 + sel) * D_;
        yp[f][0] = *reinterpret_cast<const f32x4*>(y + lane * 8);
        yp[f][1] = *reinterpret_cast<const f32x4*>(y + lane * 8 + 4);
        yp[f][2] = *reinterpret_cast<const f32x4*>(y + 512 + lane * 8);
        yp[f][3] = *reinterpret_cast<const f32x4*>(y + 512 + lane * 8 + 4);
    }
    const int row = idx_g[sel];
    float xv[16], lv[16], yv[16];
    load_row16(x1 + (size_t)row * D_, lane, xv);
    float rre = r[row];
    ln_row16(xv, lv);
    float rs = rbf(1.f / (1.f + __expf(-rre)));
#pragma unroll
    for (int k = 0; k < 4; ++k) {
        f32x4 sacc = {0.f, 0.f, 0.f, 0.f};
#pragma unroll
        for (int f = 0; f < 16; ++f) sacc += yp[f][k];
#pragma unroll
        for (int i = 0; i < 4; ++i) lv[k * 4 + i] = rbf(lv[k * 4 + i] + rbf(rbf(sacc[i]) * rs));
    }
#pragma unroll
    for (int i = 0; i < 16; ++i) xv[i] = rbf(xv[i] + lv[i]);
    store_row16(x2 + (size_t)row * D_, lane, xv);
    if (DO_LN) {
        ln_row16(xv, yv);
        store_row16(xln2 + (size_t)row * D_, lane, yv);
    }
}

extern "C" void kernel_launch(void* const* d_in, const int* in_sizes, int n_in,
                              void* d_out, int out_size, void* d_ws, size_t ws_size,
                              hipStream_t stream) {
    const u16* A_x = (const u16*)d_in[0];
    const u16* wqkv = (const u16*)d_in[1];
    const u16* wo = (const u16*)d_in[2];
    const u16* w_r = (const u16*)d_in[4];
    const u16* w_gate = (const u16*)d_in[5];
    const u16* w_up = (const u16*)d_in[6];
    const u16* w_down = (const u16*)d_in[7];
    u16* out = (u16*)d_out;

    char* ws = (char*)d_ws;
    size_t off = 0;
    auto alloc = [&](size_t bytes) { char* p = ws + off; off += (bytes + 255) & ~(size_t)255; return p; };
    u16* xln = (u16*)alloc((size_t)S_ * D_ * 2);
    u16* wqkvT = (u16*)alloc((size_t)3 * D_ * D_ * 2);
    u16* woT = (u16*)alloc((size_t)D_ * D_ * 2);
    u16* pq = (u16*)alloc((size_t)S_ * D_ * 2);
    float* kvp = (float*)alloc((size_t)16 * H_ * 1056 * 4);
    u16* x1 = (u16*)alloc((size_t)S_ * D_ * 2);
    u16* x2 = (u16*)alloc((size_t)S_ * D_ * 2);
    float* r = (float*)alloc((size_t)S_ * 4);
    float* gp = (float*)alloc((size_t)4 * 2 * 8 * F_ * 4);
    float* ypart = (float*)alloc((size_t)16 * 8 * D_ * 4);
    float* yfin = (float*)alloc((size_t)8 * D_ * 4);
    int* idx = (int*)alloc(64);
    int* cnt = (int*)alloc(512);
    u16* kvT = (u16*)alloc((size_t)H_ * 1024 * 2);
    float* zf = (float*)alloc((size_t)H_ * 32 * 4);

    u16* o = (u16*)alloc((size_t)S_ * D_ * 2);
    hipFuncSetAttribute((const void*)qkv_kernel, hipFuncAttributeMaxDynamicSharedMemorySize, 4 * (128 + 192) * 128);
    hipFuncSetAttribute((const void*)wo_kernel, hipFuncAttributeMaxDynamicSharedMemorySize, 6 * (128 + 64) * 128);

    prep_kernel<<<1536, 256, 0, stream>>>(A_x, wqkv, wo, xln, wqkvT, woT, cnt);
    const u16* xin = A_x;
    for (int layer = 0; layer < 2; ++layer) {
        u16* xo = (layer == 0) ? x2 : out;
        qkv_kernel<<<256, 512, 4 * (128 + 192) * 128, stream>>>(xln, wqkvT, pq, kvp);
        o_kernel<<<dim3(32, 8), 256, 0, stream>>>(pq, kvp, o);
        wo_kernel<<<256, 512, 6 * (128 + 64) * 128, stream>>>(o, woT, xin, x1);
        if (layer == 0) ln_r_kernel<true><<<512, 256, 0, stream>>>(x1, w_r, r, xo, xln);
        else ln_r_kernel<false><<<512, 256, 0, stream>>>(x1, w_r, r, xo, xln);
        gateup_kernel<<<dim3(56, 4), 256, 0, stream>>>(r, x1, w_gate, w_up, gp, idx);
        down_kernel<<<dim3(16, 16), 256, 0, stream>>>(gp, w_down, ypart);
        if (layer == 0) fixup_kernel<true><<<8, 64, 0, stream>>>(x1, r, idx, ypart, xo, xln);
        else fixup_kernel<false><<<8, 64, 0, stream>>>(x1, r, idx, ypart, xo, xln);
        xin = xo;
    }
}
```

```cpp
#include <hip/hip_runtime.h>
#include <hip/hip_bf16.h>
#include <stdint.h>

#define S_ 2048
#define D_ 1024
#define H_ 32
#define DH_ 32
#define F_ 3584
#define C_ 8

typedef unsigned short u16;
typedef __attribute__((ext_vector_type(8))) short bf16x8;
typedef __attribute__((ext_vector_type(4))) short bf16x4;
typedef __attribute__((ext_vector_type(4))) float f32x4;
typedef __attribute__((ext_vector_type(4))) unsigned int u32x4;

__device__ __forceinline__ float bf2f(u16 v) { return __uint_as_float(((unsigned)v) << 16); }
__device__ __forceinline__ u16 f2bf(float f) {
    __bf16 b = (__bf16)f;
    return __builtin_bit_cast(u16, b);
}
__device__ __forceinline__ float rbf(float f) { return bf2f(f2bf(f)); }
__device__ __forceinline__ float lo16(unsigned w) { return __uint_as_float(w << 16); }
__device__ __forceinline__ float hi16(unsigned w) { return __uint_as_float(w & 0xFFFF0000u); }

template <int CTRL, int RM>
__device__ __forceinline__ float dpp_addf(float v) {
    return v + __int_as_float(__builtin_amdgcn_update_dpp(0, __float_as_int(v), CTRL, RM, 0xf, true));
}
__device__ __forceinline__ float wave_sum(float v) {
    v = dpp_addf<0x111, 0xf>(v);
    v = dpp_addf<0x112, 0xf>(v);
    v = dpp_addf<0x114, 0xf>(v);
    v = dpp_addf<0x118, 0xf>(v);
    v = dpp_addf<0x142, 0xa>(v);
    v = dpp_addf<0x143, 0xc>(v);
    return __int_as_float(__builtin_amdgcn_readlane(__float_as_int(v), 63));
}
template <int CTRL, int RM>
__device__ __forceinline__ unsigned dpp_maxu(unsigned v) {
    unsigned o = (unsigned)__builtin_amdgcn_update_dpp(0, (int)v, CTRL, RM, 0xf, true);
    return v > o ? v : o;
}
__device__ __forceinline__ unsigned wave_maxu(unsigned v) {
    v = dpp_maxu<0x111, 0xf>(v);
    v = dpp_maxu<0x112, 0xf>(v);
    v = dpp_maxu<0x114, 0xf>(v);
    v = dpp_maxu<0x118, 0xf>(v);
    v = dpp_maxu<0x142, 0xa>(v);
    v = dpp_maxu<0x143, 0xc>(v);
    return (unsigned)__builtin_amdgcn_readlane((int)v, 63);
}

__device__ __forceinline__ void ln_row16(const float (&x)[16], float (&y)[16]) {
    float s = 0.f;
#pragma unroll
    for (int i = 0; i < 16; ++i) s += x[i];
    s = wave_sum(s);
    float mean = s * (1.0f / D_);
    float q = 0.f;
#pragma unroll
    for (int i = 0; i < 16; ++i) { float d = x[i] - mean; q += d * d; }
    q = wave_sum(q);
    float var = rbf(q * (1.0f / D_));
    float mb = rbf(mean);
    float inv = 1.0f / rbf(sqrtf(rbf(var + 1e-5f)));
#pragma unroll
    for (int i = 0; i < 16; ++i) y[i] = rbf(rbf(x[i] - mb) * inv);
}

__device__ __forceinline__ void load_row16(const u16* p, int lane, float (&x)[16]) {
    u32x4 a = *reinterpret_cast<const u32x4*>(p + lane * 8);
    u32x4 b = *reinterpret_cast<const u32x4*>(p + 512 + lane * 8);
#pragma unroll
    for (int i = 0; i < 4; ++i) { x[2 * i] = lo16(a[i]); x[2 * i + 1] = hi16(a[i]); x[8 + 2 * i] = lo16(b[i]); x[8 + 2 * i + 1] = hi16(b[i]); }
}
__device__ __forceinline__ void store_row16(u16* p, int lane, const float (&y)[16]) {
    u32x4 a, b;
#pragma unroll
    for (int i = 0; i < 4; ++i) {
        a[i] = (unsigned)f2bf(y[2 * i]) | ((unsigned)f2bf(y[2 * i + 1]) << 16);
        b[i] = (unsigned)f2bf(y[8 + 2 * i]) | ((unsigned)f2bf(y[8 + 2 * i + 1]) << 16);
    }
    *reinterpret_cast<u32x4*>(p + lane * 8) = a;
    *reinterpret_cast<u32x4*>(p + 512 + lane * 8) = b;
}

__global__ __launch_bounds__(256) void prep_kernel(const u16* __restrict__ x, const u16* __restrict__ wqkv,
                                                   const u16* __restrict__ wo, u16* __restrict__ xln,
                                                   u16* __restrict__ wqkvT, u16* __restrict__ woT) {
    __shared__ u16 tile[64][66];
    int b = blockIdx.x, tid = threadIdx.x;
    if (b < 512) {
        int row = b * 4 + (tid >> 6), lane = tid & 63;
        float xv[16], yv[16];
        load_row16(x + (size_t)row * D_, lane, xv);
        ln_row16(xv, yv);
        store_row16(xln + (size_t)row * D_, lane, yv);
        return;
    }
    b -= 512;
    const u16* src; u16* dst; int N, kt, nt; bool perm;
    if (b < 768) { src = wqkv; dst = wqkvT; N = 3072; kt = b / 48; nt = b % 48; perm = true; }
    else { b -= 768; src = wo; dst = woT; N = 1024; kt = b / 16; nt = b % 16; perm = false; }
    int k0 = kt * 64, n0 = nt * 64;
    for (int i = tid; i < 64 * 64 / 8; i += 256) {
        int kk = i >> 3, nn = (i & 7) * 8;
        u32x4 v = *reinterpret_cast<const u32x4*>(src + (size_t)(k0 + kk) * N + n0 + nn);
#pragma unroll
        for (int j = 0; j < 4; ++j) { tile[kk][nn + 2 * j] = (u16)(v[j] & 0xFFFF); tile[kk][nn + 2 * j + 1] = (u16)(v[j] >> 16); }
    }
    __syncthreads();
    for (int i = tid; i < 64 * 64 / 8; i += 256) {
        int nn = i >> 3, kk = (i & 7) * 8;
        int n = n0 + nn;
        int np;
        if (perm) {
            int part = n >> 10, hd = (n & 1023) >> 5, dd = n & 31;
            np = (hd >> 1) * 192 + (hd & 1) * 96 + part * 32 + dd;
        } else np = n;
        u32x4 v;
        if (perm) {
#pragma unroll
            for (int j = 0; j < 4; ++j) v[j] = (unsigned)tile[kk + 2 * j][nn] | ((unsigned)tile[kk + 2 * j + 1][nn] << 16);
        } else {
#pragma unroll
            for (int j = 0; j < 4; ++j) v[j] = (unsigned)tile[kk + 2 * j][nn] | ((unsigned)tile[kk + 2 * j + 1][nn] << 16);
        }
        *reinterpret_cast<u32x4*>(dst + (size_t)np * D_ + k0 + kk) = v;
    }
}

#define WAITV(n) asm volatile("s_waitcnt vmcnt(%0)" ::"n"(n) : "memory")
#define RBAR() do { asm volatile("s_waitcnt lgkmcnt(0)" ::: "memory"); __builtin_amdgcn_s_barrier(); } while (0)
template <int BN, int NST>
__device__ __forceinline__ void gemm_main(const u16* __restrict__ A, const u16* __restrict__ Bt, char* smem,
                                          f32x4 (&acc)[2][BN / 32]) {
    constexpr int NT = BN / 32;
    constexpr int STAGE = (128 + BN) * 128;
    constexpr int A_CH = 2, B_CH = BN * 8 / 512, G = A_CH + B_CH;
    const int tid = threadIdx.x, lane = tid & 63, wid = tid >> 6;
    const int wm = wid >> 1, wn = wid & 1;
    const int fr = lane & 15, fq = lane >> 4;
#pragma unroll
    for (int m = 0; m < 2; ++m)
#pragma unroll
        for (int n = 0; n < NT; ++n) acc[m][n] = (f32x4){0.f, 0.f, 0.f, 0.f};
    const u16* pa[A_CH]; const u16* pb[B_CH];
#pragma unroll
    for (int i = 0; i < A_CH; ++i) { int c = tid + i * 512, r = c >> 3, ph = c & 7; pa[i] = A + (size_t)r * D_ + ((ph ^ ((r >> 1) & 7)) << 3); }
#pragma unroll
    for (int i = 0; i < B_CH; ++i) { int c = tid + i * 512, r = c >> 3, ph = c & 7; pb[i] = Bt + (size_t)r * D_ + ((ph ^ ((r >> 1) & 7)) << 3); }
    const int offA0 = (wm * 32 + fr) * 128 + ((fq ^ ((fr >> 1) & 7)) << 4);
    const int offA1 = (wm * 32 + fr) * 128 + (((4 + fq) ^ ((fr >> 1) & 7)) << 4);
    const int offB0 = 16384 + (wn * (BN / 2) + fr) * 128 + ((fq ^ ((fr >> 1) & 7)) << 4);
    const int offB1 = 16384 + (wn * (BN / 2) + fr) * 128 + (((4 + fq) ^ ((fr >> 1) & 7)) << 4);
    auto issue = [&](int kt) {
        char* buf = smem + (kt % NST) * STAGE + wid * 1024;
#pragma unroll
        for (int i = 0; i < A_CH; ++i)
            __builtin_amdgcn_global_load_lds((const unsigned*)(pa[i] + kt * 64), (unsigned*)(buf + i * 8192), 16, 0, 0);
#pragma unroll
        for (int i = 0; i < B_CH; ++i)
            __builtin_amdgcn_global_load_lds((const unsigned*)(pb[i] + kt * 64), (unsigned*)(buf + 16384 + i * 8192), 16, 0, 0);
    };
    auto compute = [&](int kt) {
        const char* cur = smem + (kt % NST) * STAGE;
        bf16x8 af[2][2], bfr[2][NT];
#pragma unroll
        for (int m = 0; m < 2; ++m) { af[0][m] = *reinterpret_cast<const bf16x8*>(cur + offA0 + m * 2048); af[1][m] = *reinterpret_cast<const bf16x8*>(cur + offA1 + m * 2048); }
#pragma unroll
        for (int n = 0; n < NT; ++n) { bfr[0][n] = *reinterpret_cast<const bf16x8*>(cur + offB0 + n * 2048); bfr[1][n] = *reinterpret_cast<const bf16x8*>(cur + offB1 + n * 2048); }
#pragma unroll
        for (int ks = 0; ks < 2; ++ks)
#pragma unroll
            for (int n = 0; n < NT; ++n)
#pragma unroll
                for (int m = 0; m < 2; ++m)
                    acc[m][n] = __builtin_amdgcn_mfma_f32_16x16x32_bf16(af[ks][m], bfr[ks][n], acc[m][n], 0, 0, 0);
    };
#pragma unroll
    for (int t = 0; t < NST - 1; ++t) issue(t);
    for (int kt = 0; kt < 16 - (NST - 1); ++kt) {
        WAITV((NST - 2) * G);
        RBAR();
        issue(kt + NST - 1);
        compute(kt);
    }
#pragma unroll
    for (int j = 0; j < NST - 1; ++j) {
        WAITV((NST - 2 - j) * G);
        RBAR();
        compute(16 - (NST - 1) + j);
    }
    RBAR();
}

__global__ __launch_bounds__(512) void qkv_kernel(const u16* __restrict__ xln, const u16* __restrict__ wqkvT,
                                                  u16* __restrict__ pq, float* __restrict__ kvp) {
    extern __shared__ __attribute__((aligned(16))) char smem[];
    const int xcd = blockIdx.x & 7, jj = blockIdx.x >> 3;
    const int bm = (xcd & 3) * 4 + (jj & 3), bn = (xcd >> 2) * 8 + (jj >> 2);
    const int tid = threadIdx.x, lane = tid & 63, wid = tid >> 6;
    const int wq = wid >> 1;
    const int wn = wid & 1;
    const int fr = lane & 15, fq = lane >> 4;
    f32x4 acc[2][6];
    gemm_main<192, 4>(xln + (size_t)bm * 128 * D_, wqkvT + (size_t)bn * 192 * D_, smem, acc);

    u16* qs = reinterpret_cast<u16*>(smem + 40960);
    bf16x4 pk[2][2], vv[2][2];
    float zs[2] = {0.f, 0.f};
#pragma unroll
    for (int m = 0; m < 2; ++m) {
#pragma unroll
        for (int n = 0; n < 2; ++n) {
#pragma unroll
            for (int j = 0; j < 4; ++j) {
                float t = rbf(acc[m][n][j]);
                float p = (t > 0.f) ? rbf(t + 1.f) : rbf(__expf(t));
                p = rbf(p + 1e-6f);
                qs[(wq * 32 + m * 16 + fq * 4 + j) * 64 + wn * 32 + n * 16 + fr] = f2bf(p);
                float tk = rbf(acc[m][2 + n][j]);
                float pkk = (tk > 0.f) ? rbf(tk + 1.f) : rbf(__expf(tk));
                pkk = rbf(pkk + 1e-6f);
                pk[m][n][j] = (short)f2bf(pkk);
                zs[n] += pkk;
                vv[m][n][j] = (short)f2bf(acc[m][4 + n][j]);
            }
        }
    }
    f32x4 kv[2][2];
#pragma unroll
    for (int i = 0; i < 2; ++i)
#pragma unroll
        for (int j = 0; j < 2; ++j) {
            kv[i][j] = (f32x4){0.f, 0.f, 0.f, 0.f};
#pragma unroll
            for (int m = 0; m < 2; ++m)
                kv[i][j] = __builtin_amdgcn_mfma_f32_16x16x16bf16_1k(pk[m][i], vv[m][j], kv[i][j], 0, 0, 0);
        }
#pragma unroll
    for (int n = 0; n < 2; ++n) { zs[n] += __shfl_xor(zs[n], 16, 64); zs[n] += __shfl_xor(zs[n], 32, 64); }
    float* red = reinterpret_cast<float*>(smem);
#pragma unroll
    for (int i = 0; i < 2; ++i)
#pragma unroll
        for (int j = 0; j < 2; ++j)
#pragma unroll
            for (int r = 0; r < 4; ++r)
                red[(wq * 2 + wn) * 1056 + (i * 16 + fq * 4 + r) * 32 + j * 16 + fr] = kv[i][j][r];
    if (fq == 0) { red[(wq * 2 + wn) * 1056 + 1024 + fr] = zs[0]; red[(wq * 2 + wn) * 1056 + 1024 + 16 + fr] = zs[1]; }
    __syncthreads();
    for (int i = tid; i < 2 * 1056; i += 512) {
        int hh = i / 1056, e = i % 1056;
        float s = red[(0 * 2 + hh) * 1056 + e] + red[(1 * 2 + hh) * 1056 + e] + red[(2 * 2 + hh) * 1056 + e] + red[(3 * 2 + hh) * 1056 + e];
        kvp[((size_t)bm * H_ + bn * 2 + hh) * 1056 + e] = s;
    }
#pragma unroll
    for (int i = 0; i < 2; ++i) {
        int c = tid + i * 512, r = c >> 3, ch = c & 7;
        *reinterpret_cast<u32x4*>(pq + (size_t)(bm * 128 + r) * D_ + bn * 64 + ch * 8) = *reinterpret_cast<const u32x4*>(qs + r * 64 + ch * 8);
    }
}

__global__ __launch_bounds__(256) void o_kernel(const u16* __restrict__ pq, const float* __restrict__ kvp, u16* __restrict__ o) {
    __shared__ __attribute__((aligned(16))) u16 kvT[32][40];
    __shared__ __attribute__((aligned(16))) u16 zb[32];
    const int head = blockIdx.x, tid = threadIdx.x, lane = tid & 63, wid = tid >> 6;
    const int fr = lane & 15, fq = lane >> 4;
    u32x4 qv[4];
#pragma unroll
    for (int it = 0; it < 4; ++it)
        qv[it] = *reinterpret_cast<const u32x4*>(pq + (size_t)(blockIdx.y * 256 + wid * 64 + it * 16 + fr) * D_ + head * 32 + fq * 8);
    float v[5][16];
#pragma unroll
    for (int it = 0; it < 5; ++it) {
        int i = tid + it * 256; i = i < 1056 ? i : 1055;
#pragma unroll
        for (int b = 0; b < 16; ++b) v[it][b] = kvp[((size_t)b * H_ + head) * 1056 + i];
    }
#pragma unroll
    for (int it = 0; it < 5; ++it) {
        int i = tid + it * 256;
        if (i < 1056) {
            float s = 0.f;
#pragma unroll
            for (int b = 0; b < 16; ++b) s += v[it][b];
            if (i < 1024) kvT[i & 31][i >> 5] = f2bf(s); else zb[i - 1024] = f2bf(s);
        }
    }
    __syncthreads();
    bf16x8 ka = *reinterpret_cast<const bf16x8*>(&kvT[fr][fq * 8]);
    bf16x8 kb = *reinterpret_cast<const bf16x8*>(&kvT[16 + fr][fq * 8]);
    bf16x8 za = *reinterpret_cast<const bf16x8*>(&zb[fq * 8]);
#pragma unroll
    for (int it = 0; it < 4; ++it) {
        int row = blockIdx.y * 256 + wid * 64 + it * 16 + fr;
        bf16x8 qf = __builtin_bit_cast(bf16x8, qv[it]);
        f32x4 z4 = {0.f, 0.f, 0.f, 0.f};
        f32x4 cz = __builtin_amdgcn_mfma_f32_16x16x32_bf16(za, qf, z4, 0, 0, 0);
        f32x4 c0 = __builtin_amdgcn_mfma_f32_16x16x32_bf16(ka, qf, z4, 0, 0, 0);
        f32x4 c1 = __builtin_amdgcn_mfma_f32_16x16x32_bf16(kb, qf, z4, 0, 0, 0);
        float den = rbf(rbf(cz[0]) + 1e-6f);
        uint2 w0, w1;
        w0.x = (unsigned)f2bf(rbf(c0[0]) / den) | ((unsigned)f2bf(rbf(c0[1]) / den) << 16);
        w0.y = (unsigned)f2bf(rbf(c0[2]) / den) | ((unsigned)f2bf(rbf(c0[3]) / den) << 16);
        w1.x = (unsigned)f2bf(rbf(c1[0]) / den) | ((unsigned)f2bf(rbf(c1[1]) / den) << 16);
        w1.y = (unsigned)f2bf(rbf(c1[2]) / den) | ((unsigned)f2bf(rbf(c1[3]) / den) << 16);
        *reinterpret_cast<uint2*>(o + (size_t)row * D_ + head * 32 + fq * 4) = w0;
        *reinterpret_cast<uint2*>(o + (size_t)row * D_ + head * 32 + 16 + fq * 4) = w1;
    }
}

__global__ __launch_bounds__(512) void wo_kernel(const u16* __restrict__ o, const u16* __restrict__ woT,
                                                 const u16* __restrict__ x, u16* __restrict__ x1) {
    extern __shared__ __attribute__((aligned(16))) char smem[];
    const int xcd = blockIdx.x & 7, jj = blockIdx.x >> 3;
    const int bm = (xcd & 3) * 4 + (jj & 3), bn = (xcd >> 2) * 8 + (jj >> 2);
    const int tid = threadIdx.x, lane = tid & 63, wid = tid >> 6;
    const int wm = wid >> 1, wn = wid & 1;
    const int fr = lane & 15, fq = lane >> 4;
    f32x4 acc[2][2];
    gemm_main<64, 6>(o + (size_t)bm * 128 * D_, woT + (size_t)bn * 64 * D_, smem, acc);
    float* cs = reinterpret_cast<float*>(smem);
#pragma unroll
    for (int m = 0; m < 2; ++m)
#pragma unroll
        for (int n = 0; n < 2; ++n)
#pragma unroll
            for (int j = 0; j < 4; ++j)
                cs[(wm * 32 + m * 16 + fq * 4 + j) * 68 + wn * 32 + n * 16 + fr] = acc[m][n][j];
    __syncthreads();
#pragma unroll
    for (int i = 0; i < 2; ++i) {
        int c = tid + i * 512, r = c >> 3, ch = c & 7;
        size_t idx = (size_t)(bm * 128 + r) * D_ + bn * 64 + ch * 8;
        u32x4 xv = *reinterpret_cast<const u32x4*>(x + idx);
        u32x4 ov;
#pragma unroll
        for (int j = 0; j < 4; ++j) {
            float a0 = rbf(cs[r * 68 + ch * 8 + 2 * j]), a1 = rbf(cs[r * 68 + ch * 8 + 2 * j + 1]);
            ov[j] = (unsigned)f2bf(lo16(xv[j]) + a0) | ((unsigned)f2bf(hi16(xv[j]) + a1) << 16);
        }
        *reinterpret_cast<u32x4*>(x1 + idx) = ov;
    }
}

template <bool DO_LN>
__global__ __launch_bounds__(256) void ln_r_kernel(const u16* __restrict__ x1, const u16* __restrict__ wr,
                                                   float* __restrict__ r, u16* __restrict__ x2, u16* __restrict__ xln2) {
    int tid = threadIdx.x, row = blockIdx.x * 4 + (tid >> 6), lane = tid & 63;
    float xv[16], yv[16], wv[16];
    load_row16(x1 + (size_t)row * D_, lane, xv);
    load_row16(wr, lane, wv);
    ln_row16(xv, yv);
    float s = 0.f;
#pragma unroll
    for (int i = 0; i < 16; ++i) s += yv[i] * wv[i];
    s = wave_sum(s);
    if (lane == 0) r[row] = rbf(s);
#pragma unroll
    for (int i = 0; i < 16; ++i) xv[i] = rbf(xv[i] + yv[i]);
    store_row16(x2 + (size_t)row * D_, lane, xv);
    if (DO_LN) {
        ln_row16(xv, yv);
        store_row16(xln2 + (size_t)row * D_, lane, yv);
    }
}

__device__ __forceinline__ void top8_wave(const float* __restrict__ r, int lane, int* idx_out) {
    unsigned v[32];
#pragma unroll
    for (int i = 0; i < 32; ++i) {
        unsigned b = __float_as_uint(r[i * 64 + lane]) >> 16;
        b = (b & 0x8000u) ? (~b & 0xFFFFu) : (b | 0x8000u);
        v[i] = (b << 16) | (65535u - (unsigned)(i * 64 + lane));
    }
#pragma unroll
    for (int k = 0; k < 8; ++k) {
        unsigned best = v[0];
#pragma unroll
        for (int i = 1; i < 32; ++i) best = v[i] > best ? v[i] : best;
        best = wave_maxu(best);
        if (lane == 0) idx_out[k] = (int)(65535u - (best & 0xFFFFu));
#pragma unroll
        for (int i = 0; i < 32; ++i) v[i] = (v[i] == best) ? 0u : v[i];
    }
}

typedef __attribute__((ext_vector_type(4))) short s16x4v;
__device__ __forceinline__ s16x4v tr_read(const void* p) {
    return __builtin_amdgcn_ds_read_tr16_b64_v4i16((__attribute__((address_space(3))) s16x4v*)p);
}

__global__ __launch_bounds__(256) void gateup_kernel(const float* __restrict__ r, const u16* __restrict__ x1,
                                                     const u16* __restrict__ wg, const u16* __restrict__ wu,
                                                     float* __restrict__ gp, int* __restrict__ idx_g) {
    __shared__ __attribute__((aligned(16))) char smem[65536 + 16 * 264 * 2 + 64];
    char* Wgs = smem; char* Wus = smem + 32768;
    u16* xs = reinterpret_cast<u16*>(smem + 65536);
    int* idx = reinterpret_cast<int*>(smem + 65536 + 16 * 264 * 2);
    const int tid = threadIdx.x, lane = tid & 63, wid = tid >> 6;
    const int fr = lane & 15, fq = lane >> 4;
    const int f0 = blockIdx.x * 64, dq = blockIdx.y;
    if (wid > 0) {
        for (int i = wid - 1; i < 64; i += 3) {
            int mi = i >> 5, ii = i & 31;
            const u16* w = mi ? wu : wg;
            int row = ii * 8 + (lane >> 3);
            __builtin_amdgcn_global_load_lds((const unsigned*)(w + (size_t)(dq * 256 + row) * F_ + f0 + (lane & 7) * 8),
                                             (unsigned*)(smem + mi * 32768 + ii * 1024), 16, 0, 0);
        }
    } else {
        top8_wave(r, lane, idx);
        if (blockIdx.x == 0 && dq == 0 && lane < 8) idx_g[lane] = idx[lane];
#pragma unroll
        for (int i = 0; i < 4; ++i) {
            int t = 8 + i * 2 + (lane >> 5), c = (lane & 31) * 8;
            *reinterpret_cast<u32x4*>(xs + t * 264 + c) = (u32x4){0u, 0u, 0u, 0u};
        }
    }
    __syncthreads();
    {
        u32x4 ra[2], rb[2];
#pragma unroll
        for (int tt = 0; tt < 2; ++tt) {
            const u16* rp = x1 + (size_t)idx[wid * 2 + tt] * D_;
            ra[tt] = *reinterpret_cast<const u32x4*>(rp + lane * 8);
            rb[tt] = *reinterpret_cast<const u32x4*>(rp + 512 + lane * 8);
        }
#pragma unroll
        for (int tt = 0; tt < 2; ++tt) {
            int t = wid * 2 + tt;
            float xv[16], yv[16];
#pragma unroll
            for (int i = 0; i < 4; ++i) { xv[2 * i] = lo16(ra[tt][i]); xv[2 * i + 1] = hi16(ra[tt][i]); xv[8 + 2 * i] = lo16(rb[tt][i]); xv[8 + 2 * i + 1] = hi16(rb[tt][i]); }
            ln_row16(xv, yv);
            int e0 = lane * 8 - dq * 256, e1 = 512 + lane * 8 - dq * 256;
            u32x4 pa, pb;
#pragma unroll
            for (int i = 0; i < 4; ++i) {
                pa[i] = (unsigned)f2bf(yv[2 * i]) | ((unsigned)f2bf(yv[2 * i + 1]) << 16);
                pb[i] = (unsigned)f2bf(yv[8 + 2 * i]) | ((unsigned)f2bf(yv[8 + 2 * i + 1]) << 16);
            }
            if (e0 >= 0 && e0 < 256) *reinterpret_cast<u32x4*>(xs + t * 264 + e0) = pa;
            if (e1 >= 0 && e1 < 256) *reinterpret_cast<u32x4*>(xs + t * 264 + e1) = pb;
        }
    }
    asm volatile("s_waitcnt vmcnt(0)" ::: "memory");
    __syncthreads();
    f32x4 ag = {0.f, 0.f, 0.f, 0.f}, au = {0.f, 0.f, 0.f, 0.f};
    const int q = (lane >> 2) & 3, pp = lane & 3;
#pragma unroll
    for (int ks = 0; ks < 8; ++ks) {
        int k0 = ks * 32;
        bf16x8 af = *reinterpret_cast<const bf16x8*>(xs + fr * 264 + k0 + fq * 8);
        int ro = (k0 + fq * 8 + q) * 128 + wid * 32 + pp * 8;
        s16x4v g0 = tr_read(Wgs + ro), g1 = tr_read(Wgs + ro + 512);
        s16x4v u0 = tr_read(Wus + ro), u1 = tr_read(Wus + ro + 512);
        bf16x8 bg = {g0[0], g0[1], g0[2], g0[3], g1[0], g1[1], g1[2], g1[3]};
        bf16x8 bu = {u0[0], u0[1], u0[2], u0[3], u1[0], u1[1], u1[2], u1[3]};
        ag = __builtin_amdgcn_mfma_f32_16x16x32_bf16(af, bg, ag, 0, 0, 0);
        au = __builtin_amdgcn_mfma_f32_16x16x32_bf16(af, bu, au, 0, 0, 0);
    }
    if (fq < 2) {
#pragma unroll
        for (int j = 0; j < 4; ++j) {
            gp[((size_t)(dq * 2 + 0) * 8 + fq * 4 + j) * F_ + f0 + wid * 16 + fr] = ag[j];
            gp[((size_t)(dq * 2 + 1) * 8 + fq * 4 + j) * F_ + f0 + wid * 16 + fr] = au[j];
        }
    }
}

__global__ __launch_bounds__(256) void down_kernel(const float* __restrict__ gp, const u16* __restrict__ wd,
                                                   float* __restrict__ ypart) {
    __shared__ __attribute__((aligned(16))) char smem[28672 + 16 * 232 * 2];
    char* Ws = smem; u16* hs = reinterpret_cast<u16*>(smem + 28672);
    const int tid = threadIdx.x, lane = tid & 63, wid = tid >> 6;
    const int fr = lane & 15, fq = lane >> 4;
    const int n0 = blockIdx.x * 64, fs = blockIdx.y;
    float hv[7];
#pragma unroll
    for (int i = 0; i < 7; ++i) {
        int e = tid + i * 256; int t = e / 224, f = e % 224;
        float gs = 0.f, us = 0.f;
#pragma unroll
        for (int d = 0; d < 4; ++d) {
            gs += gp[((size_t)(d * 2 + 0) * 8 + t) * F_ + fs * 224 + f];
            us += gp[((size_t)(d * 2 + 1) * 8 + t) * F_ + fs * 224 + f];
        }
        float gb = rbf(gs);
        float sg = rbf(gb / (1.f + __expf(-gb)));
        hv[i] = sg * rbf(us);
    }
#pragma unroll
    for (int i = 0; i < 7; ++i) {
        int c = tid + i * 256;
        __builtin_amdgcn_global_load_lds((const unsigned*)(wd + (size_t)(fs * 224 + (c >> 3)) * D_ + n0 + (c & 7) * 8), (unsigned*)(Ws + i * 4096 + wid * 1024), 16, 0, 0);
    }
#pragma unroll
    for (int i = 0; i < 7; ++i) {
        int e = tid + i * 256; int t = e / 224, f = e % 224;
        hs[t * 232 + f] = f2bf(hv[i]);
        hs[(8 + t) * 232 + f] = 0;
    }
    asm volatile("s_waitcnt vmcnt(0)" ::: "memory");
    __syncthreads();
    f32x4 a = {0.f, 0.f, 0.f, 0.f};
#pragma unroll
    for (int ks = 0; ks < 7; ++ks) {
        int k0 = ks * 32;
        bf16x8 af = *reinterpret_cast<const bf16x8*>(hs + fr * 232 + k0 + fq * 8);
        int q = (lane >> 2) & 3, p = lane & 3;
        int ro = (k0 + fq * 8 + q) * 128 + wid * 32 + p * 8;
        s16x4v b0 = tr_read(Ws + ro), b1 = tr_read(Ws + ro + 512);
        bf16x8 bf = {b0[0], b0[1], b0[2], b0[3], b1[0], b1[1], b1[2], b1[3]};
        a = __builtin_amdgcn_mfma_f32_16x16x32_bf16(af, bf, a, 0, 0, 0);
    }
    if (fq < 2) {
#pragma unroll
        for (int j = 0; j < 4; ++j) ypart[((size_t)fs * 8 + fq * 4 + j) * D_ + n0 + wid * 16 + fr] = a[j];
    }
}

template <bool DO_LN>
__global__ __launch_bounds__(64) void fixup_kernel(const u16* __restrict__ x1, const float* __restrict__ r,
                                                   const int* __restrict__ idx_g, const float* __restrict__ ypart,
                                                   u16* __restrict__ x2, u16* __restrict__ xln2) {
    const int sel = blockIdx.x, lane = threadIdx.x;
    f32x4 yp[16][4];
#pragma unroll
    for (int f = 0; f < 16; ++f) {
        const float* y = ypart + ((size_t)f * 8 + sel) * D_;
        yp[f][0] = *reinterpret_cast<const f32x4*>(y + lane * 8);
        yp[f][1] = *reinterpret_cast<const f32x4*>(y + lane * 8 + 4);
        yp[f][2] = *reinterpret_cast<const f32x4*>(y + 512 + lane * 8);
        yp[f][3] = *reinterpret_cast<const f32x4*>(y + 512 + lane * 8 + 4);
    }
    const int row = idx_g[sel];
    float xv[16], lv[16], yv[16];
    load_row16(x1 + (size_t)row * D_, lane, xv);
    float rre = r[row];
    ln_row16(xv, lv);
    float rs = rbf(1.f / (1.f + __expf(-rre)));
#pragma unroll
    for (int k = 0; k < 4; ++k) {
        f32x4 sacc = {0.f, 0.f, 0.f, 0.f};
#pragma unroll
        for (int f = 0; f < 16; ++f) sacc += yp[f][k];
#pragma unroll
        for (int i = 0; i < 4; ++i) lv[k * 4 + i] = rbf(lv[k * 4 + i] + rbf(rbf(sacc[i]) * rs));
    }
#pragma unroll
    for (int i = 0; i < 16; ++i) xv[i] = rbf(xv[i] + lv[i]);
    store_row16(x2 + (size_t)row * D_, lane, xv);
    if (DO_LN) {
        ln_row16(xv, yv);
        store_row16(xln2 + (size_t)row * D_, lane, yv);
    }
}

extern "C" void kernel_launch(void* const* d_in, const int* in_sizes, int n_in,
                              void* d_out, int out_size, void* d_ws, size_t ws_size,
                              hipStream_t stream) {
    const u16* A_x = (const u16*)d_in[0];
    const u16* wqkv = (const u16*)d_in[1];
    const u16* wo = (const u16*)d_in[2];
    const u16* w_r = (const u16*)d_in[4];
    const u16* w_gate = (const u16*)d_in[5];
    const u16* w_up = (const u16*)d_in[6];
    const u16* w_down = (const u16*)d_in[7];
    u16* out = (u16*)d_out;

    char* ws = (char*)d_ws;
    size_t off = 0;
    auto alloc = [&](size_t bytes) { char* p = ws + off; off += (bytes + 255) & ~(size_t)255; return p; };
    u16* xln = (u16*)alloc((size_t)S_ * D_ * 2);
    u16* wqkvT = (u16*)alloc((size_t)3 * D_ * D_ * 2);
    u16* woT = (u16*)alloc((size_t)D_ * D_ * 2);
    u16* pq = (u16*)alloc((size_t)S_ * D_ * 2);
    float* kvp = (float*)alloc((size_t)16 * H_ * 1056 * 4);
    u16* x1 = (u16*)alloc((size_t)S_ * D_ * 2);
    u16* x2 = (u16*)alloc((size_t)S_ * D_ * 2);
    float* r = (float*)alloc((size_t)S_ * 4);
    float* gp = (float*)alloc((size_t)4 * 2 * 8 * F_ * 4);
    float* ypart = (float*)alloc((size_t)16 * 8 * D_ * 4);
    int* idx = (int*)alloc(64);

    u16* o = (u16*)alloc((size_t)S_ * D_ * 2);
    hipFuncSetAttribute((const void*)qkv_kernel, hipFuncAttributeMaxDynamicSharedMemorySize, 4 * (128 + 192) * 128);
    hipFuncSetAttribute((const void*)wo_kernel, hipFuncAttributeMaxDynamicSharedMemorySize, 6 * (128 + 64) * 128);

    prep_kernel<<<1536, 256, 0, stream>>>(A_x, wqkv, wo, xln, wqkvT, woT);
    const u16* xin = A_x;
    for (int layer = 0; layer < 2; ++layer) {
        u16* xo = (layer == 0) ? x2 : out;
        qkv_kernel<<<256, 512, 4 * (128 + 192) * 128, stream>>>(xln, wqkvT, pq, kvp);
        o_kernel<<<dim3(32, 8), 256, 0, stream>>>(pq, kvp, o);
        wo_kernel<<<256, 512, 6 * (128 + 64) * 128, stream>>>(o, woT, xin, x1);
        if (layer == 0) ln_r_kernel<true><<<512, 256, 0, stream>>>(x1, w_r, r, xo, xln);
        else ln_r_kernel<false><<<512, 256, 0, stream>>>(x1, w_r, r, xo, xln);
        gateup_kernel<<<dim3(56, 4), 256, 0, stream>>>(r, x1, w_gate, w_up, gp, idx);
        down_kernel<<<dim3(16, 16), 256, 0, stream>>>(gp, w_down, ypart);
        if (layer == 0) fixup_kernel<true><<<8, 64, 0, stream>>>(x1, r, idx, ypart, xo, xln);
        else fixup_kernel<false><<<8, 64, 0, stream>>>(x1, r, idx, ypart, xo, xln);
        xin = xo;
    }
}
```
